```python
import torch

def _softmax(x, axis=-1):
    return torch.softmax(x, dim=axis)
def _gelu(x):
    return torch.nn.functional.gelu(x, approximate="tanh")
def _silu(x):
    return torch.nn.functional.silu(x)
def _sigmoid(x):
    return torch.sigmoid(x)
def _layernorm(x, axis=-1, eps=1e-5):
    m = x.mean(dim=axis, keepdim=True)
    v = x.var(dim=axis, keepdim=True, unbiased=False)
    return (x - m) / torch.sqrt(v + eps)
def _scatter_add(tgt, idx, src, axis=0):
    return tgt.clone().index_add_(axis, idx, src)
def _segment_sum(src, seg, n):
    out = torch.zeros((n,) + src.shape[1:], dtype=src.dtype)
    return out.index_add_(0, seg, src)
def _pad(x, pw):
    flat = tuple(v for pair in reversed(pw) for v in pair)
    return torch.nn.functional.pad(x, flat)

kernel_name = 'gated_mix__linear_attn_block__glu_gated_bilinear__prompt'

def setup_inputs():

    _w = (lambda a: (lambda t: t.to(torch.bfloat16) if t.is_floating_point() else t)(torch.as_tensor(a)))
    def _A_setup():
        B, S, D = 1, 2048, 1024
        return {
            "x": _w((lambda *s: torch.randn(*s, dtype=torch.bfloat16) * 0.05)(B, S, D)),
            "wqkv": _w((lambda *s: torch.randn(*s, dtype=torch.bfloat16) * 0.8)(D, 3 * D)),
            "wo": _w((lambda *s: torch.randn(*s, dtype=torch.bfloat16) * 0.05)(D, D)),
        }
    _A_in = _A_setup()

    def _B_setup():
        B, S, D, F = 1, 2048, 1024, 3584
        return {
            "x": _w((lambda *s: torch.randn(*s, dtype=torch.bfloat16) * 0.05)(B, S, D)),
            "w_gate": _w((lambda *s: torch.randn(*s, dtype=torch.bfloat16) * 1.6)(D, F) * (D ** -0.5)),
            "w_up": _w((lambda *s: torch.randn(*s, dtype=torch.bfloat16) * 1.6)(D, F) * (D ** -0.5)),
            "w_down": _w((lambda *s: torch.randn(*s, dtype=torch.bfloat16) * 1.6)(F, D) * (F ** -0.5)),
        }
    _B_in = _B_setup()

    return {
        "A_x": _A_in["x"],
        "A_wqkv": _A_in["wqkv"],
        "A_wo": _A_in["wo"],
        "B_x": _B_in["x"],
        "B_w_gate": _B_in["w_gate"],
        "B_w_up": _B_in["w_up"],
        "B_w_down": _B_in["w_down"],
        "gate_w": _w((lambda *s: torch.randn(*s, dtype=torch.bfloat16) * 0.05)(1024) * 16.0),
    }

def reference(A_x, A_wqkv, A_wo, B_x, B_w_gate, B_w_up, B_w_down, gate_w):
    def _A_ref(x, wqkv, wo):
        B, S, D = x.shape
        H = 32; Dh = D // H
        qkv = (lambda x, *d: x.permute(*d))((x @ wqkv).reshape(B, S, 3, H, Dh), 2, 0, 3, 1, 4)
        q, k, v = qkv[0], qkv[1], qkv[2]
        phi = lambda t: torch.where(t > 0, t + 1, torch.exp(t)) + 1e-6
        pq, pk = phi(q), phi(k)
        kv = torch.einsum("bhsd,bhse->bhde", pk, v)
        z = torch.einsum("bhsd->bhd", pk)
        num = torch.einsum("bhqd,bhde->bhqe", pq, kv)
        den = torch.einsum("bhqd,bhd->bhq", pq, z)[..., None] + 1e-6
        o = num / den
        return (lambda x, *d: x.permute(*d))(o, 0, 2, 1, 3).reshape(B, S, D) @ wo

    def _B_ref(x, w_gate, w_up, w_down):
        g = torch.einsum("bsd,df->bsf", x, w_gate)
        u = torch.einsum("bsd,df->bsf", x, w_up)
        h = g * u
        return torch.einsum("bsf,fd->bsd", h, w_down)

    x = A_x
    g = _sigmoid((x * gate_w).sum(-1, keepdims=True))
    a = _A_ref(x, A_wqkv, A_wo)
    b = _B_ref(x, B_w_gate, B_w_up, B_w_down)
    return g * a + (1 - g) * b
```

```cpp
#include <hip/hip_runtime.h>
#include <hip/hip_bf16.h>
#include <stdint.h>

typedef __attribute__((ext_vector_type(8))) short bf16x8;
typedef __attribute__((ext_vector_type(4))) short s16x4;
typedef __attribute__((ext_vector_type(4))) float f32x4;
typedef __attribute__((ext_vector_type(4))) unsigned u32x4;
typedef __attribute__((ext_vector_type(2))) unsigned u32x2;
typedef __attribute__((ext_vector_type(2))) float f32x2;
typedef __attribute__((ext_vector_type(2))) __bf16 bf16x2_t;
typedef __attribute__((address_space(3))) s16x4* lds_s16x4_p;
typedef __attribute__((address_space(3))) void* lds_void_p;
typedef __attribute__((address_space(1))) const void* glb_cvoid_p;
typedef __attribute__((address_space(1))) unsigned gu32;

#define DEV __device__ __forceinline__
#define BAR() __builtin_amdgcn_s_barrier()
#define SCHED() __builtin_amdgcn_sched_barrier(0)
#define WAIT_V(n) asm volatile("s_waitcnt vmcnt(" #n ")" ::: "memory")
#define WAIT_L(n) asm volatile("s_waitcnt lgkmcnt(" #n ")" ::: "memory")
#define MFMA(a, b, c) __builtin_amdgcn_mfma_f32_16x16x32_bf16(a, b, c, 0, 0, 0)

constexpr int SEQ = 2048, DM = 1024, FF = 3584, KA2 = 4608;

DEV unsigned pack2(float a, float b) {
  f32x2 v = {a, b};
  bf16x2_t r = __builtin_convertvector(v, bf16x2_t);
  return __builtin_bit_cast(unsigned, r);
}
DEV float bfr(float a) { return (float)(__bf16)a; }
DEV float lo_f(unsigned u) { return __builtin_bit_cast(float, u << 16); }
DEV float hi_f(unsigned u) { return __builtin_bit_cast(float, u & 0xffff0000u); }
DEV float phi_f(float acc) {
  float t = bfr(acc);
  float y = t > 0.f ? bfr(t + 1.0f) : bfr(__expf(t));
  return y + 1e-6f;
}
DEV void glds16(const void* g, unsigned lds_addr) {
  __builtin_amdgcn_global_load_lds((glb_cvoid_p)g, (lds_void_p)(uintptr_t)lds_addr, 16, 0, 0);
}
DEV bf16x8 lds_b128(unsigned addr) { return *(__attribute__((address_space(3))) bf16x8*)(uintptr_t)addr; }
DEV s16x4 lds_tr(unsigned addr) {
  return __builtin_amdgcn_ds_read_tr16_b64_v4i16((lds_s16x4_p)(uintptr_t)addr);
}
DEV bf16x8 cat8(s16x4 a, s16x4 b) { return __builtin_shufflevector(a, b, 0, 1, 2, 3, 4, 5, 6, 7); }

constexpr int K1_A = 32768, K1_BUF = 73728, K1_NT = DM / 64;

template <int WC> struct K1Tiles;
template <> struct K1Tiles<0> { static constexpr int T[10] = {0, 1, 2, 3, 4, 5, 6, 13, 7, 14}; };
template <> struct K1Tiles<1> { static constexpr int T[10] = {8, 15, 9, 16, 10, 17, 11, 18, 12, 19}; };

struct K1Args {
  const char* x; const char* wqkv; const char* wg; const char* wu; const char* gate_w;
  char* a2; char* pq; float* kvp; float* gbuf; unsigned* flags;
};

template <int WC>
DEV void k1_body(const K1Args& p, unsigned sbase, int lane, int w, int r, int c) {
  const int wm = w & 3;
  const int g = lane >> 4, l15 = lane & 15;
  const int li = lane >> 3, pc = lane & 7;
  const char* xbase = p.x + (size_t)(256 * r + 32 * w) * 2048;
  unsigned voffA0 = li * 2048 + ((pc ^ (lane >> 4)) * 16);
  unsigned voffA1 = voffA0 ^ 64;
  const char* pB[5];
  unsigned incB[5];
  {
    const int krow = 32 * (w >> 2) + 16 * ((w >> 1) & 1) + 4 * (w & 1) + (li & 3) + 8 * (li >> 2);
    const int tp = (pc >> 1) ^ (li >> 1);
#pragma unroll
    for (int G = 0; G < 5; ++G) {
      const int T = 4 * G + tp;
      if (T < 6) {
        pB[G] = p.wqkv + (size_t)krow * 6144 + ((T >> 1) * 1024 + 32 * c + 16 * (T & 1) + 8 * (pc & 1)) * 2;
        incB[G] = 64 * 6144;
      } else {
        const int sj = T < 13 ? T - 6 : T - 13;
        const int j = (c & 1) ? (sj < 2 ? sj + 5 : (sj < 6 ? sj - 1 : 0)) : sj;
        pB[G] = (T < 13 ? p.wg : p.wu) + (size_t)krow * 7168 + (112 * c + 16 * j + 8 * (pc & 1)) * 2;
        incB[G] = 64 * 7168;
      }
    }
  }
  unsigned rA[2], rB[4];
  rA[0] = (64 * wm + l15) * 128 + ((g ^ (l15 >> 1)) * 16);
  rA[1] = rA[0] ^ 64;
  {
    const int i = (g & 1) * 4 + (l15 >> 2), pp = lane & 3;
#pragma unroll
    for (int tp = 0; tp < 4; ++tp)
      rB[tp] = K1_A + (g >> 1) * 10240 + i * 128 + ((tp ^ (i >> 1)) * 2 + (pp >> 1)) * 16 + (pp & 1) * 8;
  }
  auto stageA = [&](int i, int kt, unsigned buf) {
    glds16(xbase + (size_t)i * 16384 + kt * 128 + ((i & 1) ? voffA1 : voffA0), buf + (4 * w + i) * 1024);
  };
  auto stageB = [&](int G, unsigned buf) {
    glds16(pB[G], buf + K1_A + (w * 5 + G) * 1024);
    pB[G] += incB[G];
  };

  f32x4 acc[4][10];
#pragma unroll
  for (int m = 0; m < 4; ++m)
#pragma unroll
    for (int n = 0; n < 10; ++n) acc[m][n] = (f32x4){0.f, 0.f, 0.f, 0.f};

#pragma unroll
  for (int i = 0; i < 4; ++i) stageA(i, 0, sbase);
#pragma unroll
  for (int G = 0; G < 5; ++G) stageB(G, sbase);
  if (WC == 0) {
#pragma unroll
    for (int G = 0; G < 5; ++G) stageB(G, sbase + K1_BUF);
  }
  {
    const int row = 256 * r + 8 * c + w;
    const u32x4* xr = (const u32x4*)(p.x + (size_t)row * 2048) + lane * 2;
    const u32x4* gw = (const u32x4*)p.gate_w + lane * 2;
    float s = 0.f;
#pragma unroll
    for (int q = 0; q < 2; ++q) {
      u32x4 a = xr[q], b = gw[q];
#pragma unroll
      for (int e = 0; e < 4; ++e) s += bfr(lo_f(a[e]) * lo_f(b[e])) + bfr(hi_f(a[e]) * hi_f(b[e]));
    }
#pragma unroll
    for (int o = 32; o > 0; o >>= 1) s += __shfl_xor(s, o);
    if (lane == 0) {
      float gg = bfr(1.0f / (1.0f + __expf(-bfr(s))));
      p.gbuf[row] = gg;
      p.gbuf[SEQ + row] = bfr(1.0f - gg);
    }
  }
  WAIT_V(0);
  BAR();
  if (WC == 1) BAR();

  bf16x8 af[4], bfv[5];
  for (int t = 0; t < K1_NT; ++t) {
    const unsigned cur = sbase + (t & 1) * K1_BUF, oth = sbase + ((t & 1) ^ 1) * K1_BUF;
    const bool moreA = t + 1 < K1_NT;
    const bool moreB = WC == 0 ? (t + 2 < K1_NT) : (t + 1 < K1_NT);
    const unsigned bdst = WC == 0 ? cur : oth;
#pragma unroll
    for (int ph = 0; ph < 4; ++ph) {
      const int kk = ph >> 1, nh = ph & 1;
      if (nh == 0) {
#pragma unroll
        for (int m = 0; m < 4; ++m) af[m] = lds_b128(cur + rA[kk] + m * 2048);
      }
#pragma unroll
      for (int n = 0; n < 5; ++n) {
        const int T = K1Tiles<WC>::T[nh * 5 + n];
        const unsigned a = cur + rB[T & 3] + ((kk * 4) * 5 + (T >> 2)) * 1024;
        bfv[n] = cat8(lds_tr(a), lds_tr(a + 5120));
      }
      if (ph < 2) {
        if (moreA) { stageA(2 * ph, t + 1, oth); stageA(2 * ph + 1, t + 1, oth); }
      } else if (moreB) {
        if (ph == 2) { stageB(0, bdst); stageB(1, bdst); stageB(2, bdst); }
        else { stageB(3, bdst); stageB(4, bdst); }
      }
      if (WC == 1) {
        if (ph == 1) { if (moreA) WAIT_V(4); else WAIT_V(0); }
        if (ph == 3) { if (moreB) WAIT_V(5); else WAIT_V(0); }
      }
      WAIT_L(0);
      SCHED();
      BAR();
      SCHED();
      __builtin_amdgcn_s_setprio(1);
#pragma unroll
      for (int n = 0; n < 5; ++n) {
        const int T = K1Tiles<WC>::T[nh * 5 + n];
#pragma unroll
        for (int m = 0; m < 4; ++m) {
          if (T >= 2 && T < 6) acc[m][nh * 5 + n] = MFMA(af[m], bfv[n], acc[m][nh * 5 + n]);
          else acc[m][nh * 5 + n] = MFMA(bfv[n], af[m], acc[m][nh * 5 + n]);
        }
      }
      __builtin_amdgcn_s_setprio(0);
      if (WC == 0 && ph == 3) { if (moreB) WAIT_V(5); else WAIT_V(0); }
      SCHED();
      BAR();
      SCHED();
    }
  }
  if (WC == 0) BAR();

  const int row0 = 256 * r + 64 * wm;
  {
    constexpr int NP = WC == 0 ? 1 : 2;
#pragma unroll
    for (int pr = 0; pr < NP; ++pr) {
      const int s0 = WC == 0 ? 0 : 2 + 2 * pr;
      const int n0 = WC == 0 ? 6 : 4 * pr;
      const int j0 = (c & 1) ? (s0 < 2 ? s0 + 5 : s0 - 1) : s0;
      const int J = 7 * c + j0;
#pragma unroll
      for (int m = 0; m < 4; ++m) {
        float h[8];
#pragma unroll
        for (int e = 0; e < 4; ++e) {
          h[e] = bfr(acc[m][n0][e]) * bfr(acc[m][n0 + 1][e]);
          h[4 + e] = bfr(acc[m][n0 + 2][e]) * bfr(acc[m][n0 + 3][e]);
        }
        u32x4 v = {pack2(h[0], h[1]), pack2(h[2], h[3]), pack2(h[4], h[5]), pack2(h[6], h[7])};
        *(u32x4*)(p.a2 + ((size_t)(row0 + 16 * m + l15) * KA2 + 16 * J + 8 * g) * 2) = v;
      }
    }
    if (WC == 1) {
      const int J = 7 * c + ((c & 1) ? 0 : 6);
#pragma unroll
      for (int m = 0; m < 4; ++m) {
        float h[4];
#pragma unroll
        for (int e = 0; e < 4; ++e) h[e] = bfr(acc[m][8][e]) * bfr(acc[m][9][e]);
        u32x2 v = {pack2(h[0], h[1]), pack2(h[2], h[3])};
        *(u32x2*)(p.a2 + ((size_t)(row0 + 16 * m + l15) * KA2 + 32 * (J >> 1) + 8 * g + 4 * (J & 1)) * 2) = v;
      }
    }
  }
  if (WC == 0) {
#pragma unroll
    for (int m = 0; m < 4; ++m) {
      u32x4 v = {pack2(phi_f(acc[m][0][0]), phi_f(acc[m][0][1])), pack2(phi_f(acc[m][0][2]), phi_f(acc[m][0][3])),
                 pack2(phi_f(acc[m][1][0]), phi_f(acc[m][1][1])), pack2(phi_f(acc[m][1][2]), phi_f(acc[m][1][3]))};
      *(u32x4*)(p.pq + ((size_t)((row0 >> 4) + m) * 32 + c) * 1024 + lane * 16) = v;
    }
    f32x4 kv[2][3];
#pragma unroll
    for (int dt = 0; dt < 2; ++dt)
#pragma unroll
      for (int et = 0; et < 3; ++et) kv[dt][et] = (f32x4){0.f, 0.f, 0.f, 0.f};
    const u32x4 ones = {0x3f803f80u, 0x3f803f80u, 0x3f803f80u, 0x3f803f80u};
#pragma unroll
    for (int ks = 0; ks < 2; ++ks) {
      bf16x8 kf[2], vf[3];
#pragma unroll
      for (int dt = 0; dt < 2; ++dt) {
        const f32x4 a = acc[2 * ks][2 + dt], b = acc[2 * ks + 1][2 + dt];
        u32x4 v = {pack2(phi_f(a[0]), phi_f(a[1])), pack2(phi_f(a[2]), phi_f(a[3])), pack2(phi_f(b[0]), phi_f(b[1])),
                   pack2(phi_f(b[2]), phi_f(b[3]))};
        kf[dt] = __builtin_bit_cast(bf16x8, v);
        const f32x4 a2 = acc[2 * ks][4 + dt], b2 = acc[2 * ks + 1][4 + dt];
        u32x4 v2 = {pack2(a2[0], a2[1]), pack2(a2[2], a2[3]), pack2(b2[0], b2[1]), pack2(b2[2], b2[3])};
        vf[dt] = __builtin_bit_cast(bf16x8, v2);
      }
      vf[2] = __builtin_bit_cast(bf16x8, ones);
#pragma unroll
      for (int dt = 0; dt < 2; ++dt)
#pragma unroll
        for (int et = 0; et < 3; ++et) kv[dt][et] = MFMA(kf[dt], vf[et], kv[dt][et]);
    }
#pragma unroll
    for (int dt = 0; dt < 2; ++dt)
#pragma unroll
      for (int et = 0; et < 3; ++et)
        *(__attribute__((address_space(3))) f32x4*)(uintptr_t)(sbase + ((wm * 6 + dt * 3 + et) * 64 + lane) * 16) = kv[dt][et];
  }
  __syncthreads();
  {
    const int tid = w * 64 + lane;
    if (tid < 384) {
      f32x4 s = (f32x4){0.f, 0.f, 0.f, 0.f};
#pragma unroll
      for (int q = 0; q < 4; ++q) s += *(__attribute__((address_space(3))) f32x4*)(uintptr_t)(sbase + (q * 384 + tid) * 16);
      *(f32x4*)(p.kvp + ((size_t)(r * 32 + c) * 384 + tid) * 4) = s;
    }
  }
}

__global__ __launch_bounds__(512, 2) void k1_kernel(K1Args p) {
  __shared__ __attribute__((aligned(1024))) char smem[2 * K1_BUF];
  const int lane = threadIdx.x & 63;
  const int w = __builtin_amdgcn_readfirstlane(threadIdx.x >> 6);
  const int bid = blockIdx.x;
  const int xcd = bid & 7, idx = bid >> 3;
  const int r = idx & 7, c = xcd * 4 + (idx >> 3);
  const unsigned sbase = (unsigned)(uintptr_t)(__attribute__((address_space(3))) char*)smem;
  if (bid == 0 && threadIdx.x < 512) p.flags[threadIdx.x] = 0u;
  if (w < 4) k1_body<0>(p, sbase, lane, w, r, c);
  else k1_body<1>(p, sbase, lane, w, r, c);
}

__global__ __launch_bounds__(512) void km_kernel(const char* pq, const float* kvp, const float* gbuf, char* a2) {
  __shared__ __attribute__((aligned(16))) char sm[3 * 1024];
  const int tid = threadIdx.x, lane = tid & 63, w = tid >> 6;
  const int r = blockIdx.x >> 5, h = blockIdx.x & 31;
  const int g = lane >> 4, l15 = lane & 15;
  bf16x8 pf[2];
  float gv[2];
#pragma unroll
  for (int m = 0; m < 2; ++m) {
    pf[m] = *(const bf16x8*)(pq + ((size_t)(16 * r + 2 * w + m) * 32 + h) * 1024 + lane * 16);
    gv[m] = gbuf[256 * r + 32 * w + 16 * m + l15];
  }
  if (tid < 384) {
    f32x4 s = (f32x4){0.f, 0.f, 0.f, 0.f};
#pragma unroll
    for (int q = 0; q < 8; ++q) s += *(const f32x4*)(kvp + ((size_t)(q * 32 + h) * 384 + tid) * 4);
    const int tt = tid >> 6, dt = tt / 3, et = tt - 3 * dt;
    u32x2 v = {pack2(s[0], s[1]), pack2(s[2], s[3])};
    *(u32x2*)(sm + (et * 64 + lane) * 16 + dt * 8) = v;
  }
  __syncthreads();
  bf16x8 kf[3];
#pragma unroll
  for (int et = 0; et < 3; ++et) kf[et] = *(const bf16x8*)(sm + (et * 64 + lane) * 16);
#pragma unroll
  for (int m = 0; m < 2; ++m) {
    const f32x4 z = {0.f, 0.f, 0.f, 0.f};
    f32x4 n0 = MFMA(kf[0], pf[m], z), n1 = MFMA(kf[1], pf[m], z), dn = MFMA(kf[2], pf[m], z);
    const float den = bfr(bfr(dn[0]) + 1e-6f);
    const float sc = gv[m] / den;
    u32x4 v = {pack2(bfr(n0[0]) * sc, bfr(n0[1]) * sc), pack2(bfr(n0[2]) * sc, bfr(n0[3]) * sc),
               pack2(bfr(n1[0]) * sc, bfr(n1[1]) * sc), pack2(bfr(n1[2]) * sc, bfr(n1[3]) * sc)};
    *(u32x4*)(a2 + ((size_t)(256 * r + 32 * w + 16 * m + l15) * KA2 + FF + 32 * h + 8 * g) * 2) = v;
  }
}

constexpr int K2_SUB = 32768, K2_NS = 36;

struct K2Args {
  const char* a2; const char* wd; const char* wo; const float* gbuf;
  float* xch; unsigned* flags; char* out;
};

__global__ __launch_bounds__(512, 2) void k2_kernel(K2Args p) {
  __shared__ __attribute__((aligned(1024))) char smem[4 * K2_SUB];
  const int lane = threadIdx.x & 63;
  const int w = __builtin_amdgcn_readfirstlane(threadIdx.x >> 6);
  const int grp = w >> 2, wm = w & 1, wn = (w >> 1) & 1;
  const int bid = blockIdx.x;
  const int xcd = bid & 7, idx = bid >> 3;
  const int s = idx & 1, tn = (idx >> 1) & 7, tm = xcd * 2 + (idx >> 4);
  const int tile = tm * 8 + tn;
  const unsigned sbase = (unsigned)(uintptr_t)(__attribute__((address_space(3))) char*)smem;
  const int g = lane >> 4, l15 = lane & 15, li = lane >> 3, pc = lane & 7;
  const int wq = w & 3;
  const char* abase = p.a2 + ((size_t)(128 * tm + 32 * wq) * KA2 + s * 2304) * 2;
  const unsigned voffA0 = li * (KA2 * 2) + ((pc ^ (lane >> 4)) * 16), voffA1 = voffA0 ^ 64;
  const unsigned voffB = li * 2048 + (((pc >> 1) ^ (li >> 1)) * 32) + (pc & 1) * 16;
  auto stage = [&](int j) {
    const unsigned buf = sbase + (j & 3) * K2_SUB;
    const int k0 = s * 2304 + j * 64;
    const char* bsrc = (k0 < FF ? p.wd + (size_t)k0 * 2048 : p.wo + (size_t)(k0 - FF) * 2048) + tn * 256 + wq * 32768;
#pragma unroll
    for (int i = 0; i < 4; ++i)
      glds16(abase + (size_t)i * (8 * KA2 * 2) + j * 128 + ((i & 1) ? voffA1 : voffA0), buf + (4 * wq + i) * 1024);
#pragma unroll
    for (int i = 0; i < 4; ++i)
      glds16(bsrc + (i >> 1) * 16384 + (i & 1) * 128 + voffB, buf + 16384 + (4 * wq + i) * 1024);
  };
  unsigned rA[2], rB[4];
  rA[0] = (64 * wm + l15) * 128 + ((g ^ (l15 >> 1)) * 16);
  rA[1] = rA[0] ^ 64;
  {
    const int i = (g & 1) * 4 + (l15 >> 2), pp = lane & 3;
#pragma unroll
    for (int tp = 0; tp < 4; ++tp)
      rB[tp] = 16384 + ((g >> 1) * 2 + wn) * 1024 + i * 128 + ((tp ^ (i >> 1)) * 2 + (pp >> 1)) * 16 + (pp & 1) * 8;
  }
  float omg[4];
#pragma unroll
  for (int m = 0; m < 4; ++m) omg[m] = p.gbuf[SEQ + 128 * tm + 64 * wm + 16 * m + l15];

  f32x4 acc[4][4];
#pragma unroll
  for (int m = 0; m < 4; ++m)
#pragma unroll
    for (int n = 0; n < 4; ++n) acc[m][n] = (f32x4){0.f, 0.f, 0.f, 0.f};

  stage(grp);
  stage(grp + 2);
  WAIT_V(8);
  BAR();
  if (grp == 1) BAR();
  for (int j = grp; j < K2_NS; j += 2) {
    const unsigned cur = sbase + (j & 3) * K2_SUB;
    bf16x8 af[2][4], bfv[2][4];
#pragma unroll
    for (int kk = 0; kk < 2; ++kk) {
#pragma unroll
      for (int m = 0; m < 4; ++m) af[kk][m] = lds_b128(cur + rA[kk] + m * 2048);
#pragma unroll
      for (int n = 0; n < 4; ++n) {
        const unsigned a = cur + rB[n] + kk * 8192;
        bfv[kk][n] = cat8(lds_tr(a), lds_tr(a + 4096));
      }
    }
    if (j >= 1 && j + 3 < K2_NS) { stage(j + 3); WAIT_V(8); } else { WAIT_V(0); }
    WAIT_L(0);
    SCHED();
    BAR();
    SCHED();
    __builtin_amdgcn_s_setprio(1);
#pragma unroll
    for (int kk = 0; kk < 2; ++kk)
#pragma unroll
      for (int n = 0; n < 4; ++n)
#pragma unroll
        for (int m = 0; m < 4; ++m) acc[m][n] = MFMA(bfv[kk][n], af[kk][m], acc[m][n]);
    __builtin_amdgcn_s_setprio(0);
    if (j >= (s ? 18 : 34) && j < (s ? 20 : 36)) {
#pragma unroll
      for (int m = 0; m < 4; ++m)
#pragma unroll
        for (int n = 0; n < 4; ++n) acc[m][n] *= omg[m];
    }
    SCHED();
    BAR();
    SCHED();
  }
  if (grp == 0) BAR();

  {
    const unsigned wb = sbase + (wq * 2 + grp) * 8192 + lane * 16;
    const unsigned rb = sbase + (wq * 2 + (grp ^ 1)) * 8192 + lane * 16;
    f32x4 fin[2][4];
    if (grp) {
#pragma unroll
      for (int q = 0; q < 8; ++q) {
        *(__attribute__((address_space(3))) f32x4*)(uintptr_t)(wb + q * 1024) = acc[q >> 2][q & 3];
        fin[q >> 2][q & 3] = acc[2 + (q >> 2)][q & 3];
      }
    } else {
#pragma unroll
      for (int q = 0; q < 8; ++q) {
        *(__attribute__((address_space(3))) f32x4*)(uintptr_t)(wb + q * 1024) = acc[2 + (q >> 2)][q & 3];
        fin[q >> 2][q & 3] = acc[q >> 2][q & 3];
      }
    }
    __syncthreads();
#pragma unroll
    for (int q = 0; q < 8; ++q)
      fin[q >> 2][q & 3] += *(__attribute__((address_space(3))) f32x4*)(uintptr_t)(rb + q * 1024);
    const int slot = wn * 2 + grp;
    if (wm != s) {
      float* dst = p.xch + ((size_t)(tile * 2 + s) * 4 + slot) * 2048 + lane * 4;
#pragma unroll
      for (int q = 0; q < 8; ++q) {
        f32x4 v = fin[q >> 2][q & 3];
        asm volatile("global_store_dwordx4 %0, %1, off sc1\n\ts_nop 1" ::"v"(dst + q * 256), "v"(v) : "memory");
      }
      WAIT_V(0);
    }
    __syncthreads();
    if (threadIdx.x == 0)
      __hip_atomic_store((gu32*)(p.flags + 16 + tile * 2 + s), 1u, __ATOMIC_RELAXED, __HIP_MEMORY_SCOPE_AGENT);
    if (wm == s) {
      gu32* f = (gu32*)(p.flags + 16 + tile * 2 + (s ^ 1));
      while (__hip_atomic_load(f, __ATOMIC_RELAXED, __HIP_MEMORY_SCOPE_AGENT) == 0u) __builtin_amdgcn_s_sleep(1);
      const float* src = p.xch + ((size_t)(tile * 2 + (s ^ 1)) * 4 + slot) * 2048 + lane * 4;
      f32x4 o[8];
#pragma unroll
      for (int q = 0; q < 8; ++q) asm volatile("global_load_dwordx4 %0, %1, off sc1" : "=&v"(o[q]) : "v"(src + q * 256) : "memory");
      WAIT_V(0);
      SCHED();
#pragma unroll
      for (int q = 0; q < 8; ++q) {
        asm volatile("" : "+v"(o[q]));
        const int mm = q >> 2, n = q & 3;
        f32x4 v = fin[mm][n] + o[q];
        u32x2 pk = {pack2(v[0], v[1]), pack2(v[2], v[3])};
        const int row = 128 * tm + 64 * wm + 16 * (2 * grp + mm) + l15;
        *(u32x2*)(p.out + ((size_t)row * DM + 128 * tn + 64 * wn + 16 * n + 4 * g) * 2) = pk;
      }
    }
  }
}

extern "C" void kernel_launch(void* const* d_in, const int* in_sizes, int n_in, void* d_out, int out_size, void* d_ws,
                              size_t ws_size, hipStream_t stream) {
  char* ws = (char*)d_ws;
  unsigned* flags = (unsigned*)ws;
  float* gbuf = (float*)(ws + 4096);
  float* kvp = (float*)(ws + 65536);
  char* pq = ws + (2u << 20);
  char* a2 = ws + (8u << 20);
  float* xch = (float*)(ws + (32u << 20));
  K1Args a;
  a.x = (const char*)d_in[0]; a.wqkv = (const char*)d_in[1]; a.wg = (const char*)d_in[4]; a.wu = (const char*)d_in[5];
  a.gate_w = (const char*)d_in[7]; a.a2 = a2; a.pq = pq; a.kvp = kvp; a.gbuf = gbuf; a.flags = flags;
  k1_kernel<<<256, 512, 0, stream>>>(a);
  km_kernel<<<256, 512, 0, stream>>>(pq, kvp, gbuf, a2);
  K2Args b;
  b.a2 = a2; b.wd = (const char*)d_in[6]; b.wo = (const char*)d_in[2]; b.gbuf = gbuf; b.xch = xch; b.flags = flags;
  b.out = (char*)d_out;
  k2_kernel<<<256, 512, 0, stream>>>(b);
}
```

```cpp
#include <hip/hip_runtime.h>
#include <hip/hip_bf16.h>
#include <stdint.h>

typedef __attribute__((ext_vector_type(8))) short bf16x8;
typedef __attribute__((ext_vector_type(4))) short s16x4;
typedef __attribute__((ext_vector_type(4))) float f32x4;
typedef __attribute__((ext_vector_type(4))) unsigned u32x4;
typedef __attribute__((ext_vector_type(2))) unsigned u32x2;
typedef __attribute__((ext_vector_type(2))) float f32x2;
typedef __attribute__((ext_vector_type(2))) __bf16 bf16x2_t;
typedef __attribute__((address_space(3))) s16x4* lds_s16x4_p;
typedef __attribute__((address_space(3))) void* lds_void_p;
typedef __attribute__((address_space(1))) const void* glb_cvoid_p;
typedef __attribute__((address_space(1))) unsigned gu32;

#define DEV __device__ __forceinline__
#define BAR() __builtin_amdgcn_s_barrier()
#define SCHED() __builtin_amdgcn_sched_barrier(0)
#define WAIT_V(n) asm volatile("s_waitcnt vmcnt(" #n ")" ::: "memory")
#define WAIT_L(n) asm volatile("s_waitcnt lgkmcnt(" #n ")" ::: "memory")
#define MFMA(a, b, c) __builtin_amdgcn_mfma_f32_16x16x32_bf16(a, b, c, 0, 0, 0)

#define ST(k, i)
constexpr int SEQ = 2048, DM = 1024, FF = 3584, KA2 = 4608;

DEV unsigned pack2(float a, float b) {
  f32x2 v = {a, b};
  bf16x2_t r = __builtin_convertvector(v, bf16x2_t);
  return __builtin_bit_cast(unsigned, r);
}
DEV float bfr(float a) { return (float)(__bf16)a; }
DEV float lo_f(unsigned u) { return __builtin_bit_cast(float, u << 16); }
DEV float hi_f(unsigned u) { return __builtin_bit_cast(float, u & 0xffff0000u); }
DEV unsigned phi2(float a, float b) {
  const float ya = a > 0.f ? a + 1.0f : __expf(a), yb = b > 0.f ? b + 1.0f : __expf(b);
  return pack2(ya + 1e-6f, yb + 1e-6f);
}
DEV unsigned mul2(float g0, float g1, float u0, float u1) { return pack2(g0 * u0, g1 * u1); }
DEV void glds_s(const char* ubase, unsigned voff, unsigned lds_addr) {
  asm volatile("s_mov_b32 m0, %2\n\ts_nop 0\n\tglobal_load_lds_dwordx4 %0, %1" ::"v"(voff), "s"(ubase), "s"(lds_addr)
               : "memory");
}
DEV void glds_m(const char* ubase, unsigned voff, unsigned lds_addr, int em) {
  asm volatile("s_mov_b32 m0, %2\n\ts_or_b32 exec_lo, %3, 1\n\ts_mov_b32 exec_hi, %3\n\tglobal_load_lds_dwordx4 %0, %1\n\ts_mov_b64 exec, -1" ::"v"(voff), "s"(ubase), "s"(lds_addr), "s"(em)
               : "memory", "scc");
}
DEV void glds_v(const char* vptr, unsigned lds_addr) {
  asm volatile("s_mov_b32 m0, %1\n\ts_nop 0\n\tglobal_load_lds_dwordx4 %0, off" ::"v"(vptr), "s"(lds_addr)
               : "memory");
}
DEV bf16x8 lds_b128(unsigned addr) { return *(__attribute__((address_space(3))) bf16x8*)(uintptr_t)addr; }
#define LDS_TR(dst, addr, off) asm volatile("ds_read_b64_tr_b16 %0, %1 offset:%2" : "=v"(dst) : "v"(addr), "i"(off) : "memory")
DEV bf16x8 cat8(s16x4 a, s16x4 b) { return __builtin_shufflevector(a, b, 0, 1, 2, 3, 4, 5, 6, 7); }

constexpr int K1_A = 32768, K1_BUF = 73728, K1_NT = DM / 64;

template <int WC> struct K1Tiles;
template <> struct K1Tiles<0> { static constexpr int T[10] = {2, 3, 4, 5, 6, 13, 7, 14, 12, 19}; };
template <> struct K1Tiles<1> { static constexpr int T[10] = {0, 1, 8, 15, 9, 16, 10, 17, 11, 18}; };

struct K1Args {
  const char* x; const char* wqkv; const char* wg; const char* wu; const char* gate_w;
  char* a2; char* pq; float* kvp; float* gbuf; unsigned* flags;
};

template <int WC>
DEV void k1_body(const K1Args& p, unsigned sbase, int lane, int w, int r, int c) {
  const int wm = w & 3;
  const int g = lane >> 4, l15 = lane & 15;
  const int li = lane >> 3, pc = lane & 7;
  const char* xbase = p.x + (size_t)(256 * r + 32 * w) * 2048;
  unsigned voffA0 = li * 2048 + ((pc ^ (lane >> 4)) * 16);
  unsigned voffA1 = voffA0 ^ 64;
  const char* pB1; const char* pB3;
  unsigned incB1, voffB[3];
  {
    const int krow = 32 * (w >> 2) + 16 * ((w >> 1) & 1) + 4 * (w & 1) + (li & 3) + 8 * (li >> 2);
    const int tp = (pc >> 1) ^ (li >> 1);
    auto off_qkv = [&](int T) { return (unsigned)(krow * 6144 + ((T >> 1) * 1024 + 32 * c + 16 * (T & 1) + 8 * (pc & 1)) * 2); };
    auto off_gu = [&](int sj) {
      const int j = (c & 1) ? (sj < 2 ? sj + 5 : (sj < 6 ? sj - 1 : 0)) : sj;
      return (unsigned)(krow * 7168 + (112 * c + 16 * j + 8 * (pc & 1)) * 2);
    };
    voffB[0] = off_qkv(tp);
    voffB[1] = off_gu(2 + tp);
    voffB[2] = off_gu(3 + tp);
    pB1 = tp < 2 ? p.wqkv + off_qkv(4 + tp) : p.wg + off_gu(tp - 2);
    incB1 = tp < 2 ? 64 * 6144 : 64 * 7168;
    pB3 = tp < 1 ? p.wg + off_gu(6) : p.wu + off_gu(tp - 1);
  }
  unsigned rA[2], rB[4];
  rA[0] = (64 * wm + l15) * 128 + ((g ^ (l15 >> 1)) * 16);
  rA[1] = rA[0] ^ 64;
  {
    const int i = (g & 1) * 4 + (l15 >> 2), pp = lane & 3;
#pragma unroll
    for (int tp = 0; tp < 4; ++tp)
      rB[tp] = K1_A + (g >> 1) * 10240 + i * 128 + ((tp ^ (i >> 1)) * 2 + (pp >> 1)) * 16 + (pp & 1) * 8;
  }
  auto stageA = [&](int i, int kt, unsigned buf) {
    glds_s(xbase + i * 16384 + kt * 128, (i & 1) ? voffA1 : voffA0, buf + (4 * w + i) * 1024);
  };
  auto stageB = [&](int G, int kt, unsigned buf) {
    const unsigned dst = buf + K1_A + (w * 5 + G) * 1024;
    if (G == 0) glds_s(p.wqkv + (size_t)kt * (64 * 6144), voffB[0], dst);
    if (G == 2) glds_s(p.wg + (size_t)kt * (64 * 7168), voffB[1], dst);
    if (G == 4) glds_s(p.wu + (size_t)kt * (64 * 7168), voffB[2], dst);
    if (G == 1) { glds_v(pB1, dst); pB1 += incB1; }
    if (G == 3) { glds_v(pB3, dst); pB3 += 64 * 7168; }
  };

  f32x4 acc[4][10];
#pragma unroll
  for (int m = 0; m < 4; ++m)
#pragma unroll
    for (int n = 0; n < 10; ++n) acc[m][n] = (f32x4){0.f, 0.f, 0.f, 0.f};

  ST(0, 0); ST(0, 6);
#pragma unroll
  for (int i = 0; i < 4; ++i) stageA(i, 0, sbase);
#pragma unroll
  for (int G = 0; G < 5; ++G) stageB(G, 0, sbase);
  if (WC == 0) {
#pragma unroll
    for (int G = 0; G < 5; ++G) stageB(G, 1, sbase + K1_BUF);
  }
#pragma unroll
  for (int m = 0; m < 4; ++m)
    asm volatile("" : "+v"(acc[m][0]), "+v"(acc[m][1]), "+v"(acc[m][2]), "+v"(acc[m][3]), "+v"(acc[m][4]), "+v"(acc[m][5]),
                      "+v"(acc[m][6]), "+v"(acc[m][7]), "+v"(acc[m][8]), "+v"(acc[m][9]));
  WAIT_V(5);
  BAR();
  if (WC == 1) BAR();
  ST(0, 1);

  bf16x8 af[4], bfv[5];
  int nt = K1_NT;
  asm volatile("" : "+s"(nt));
  int t = 0;
  do {
    const unsigned cur = sbase + (t & 1) * K1_BUF, oth = sbase + ((t & 1) ^ 1) * K1_BUF;
    const bool moreA = t + 1 < nt;
    const bool moreB = WC == 0 ? (t + 2 < nt) : (t + 1 < nt);
    const unsigned bdst = WC == 0 ? cur : oth;
    const int tb = WC == 0 ? t + 2 : t + 1;
    unsigned rBc[4];
#pragma unroll
    for (int q = 0; q < 4; ++q) rBc[q] = cur + rB[q];
#pragma unroll
    for (int ph = 0; ph < 4; ++ph) {
      const int kk = ph >> 1, nh = ph & 1;
      if (nh == 0) {
#pragma unroll
        for (int m = 0; m < 4; ++m) af[m] = lds_b128(cur + rA[kk] + m * 2048);
      }
      s16x4 bl[5], bh[5];
#pragma unroll
      for (int n = 0; n < 5; ++n) {
        const int T = K1Tiles<WC>::T[nh * 5 + n];
        LDS_TR(bl[n], rBc[T & 3], ((kk * 4) * 5 + (T >> 2)) * 1024);
        LDS_TR(bh[n], rBc[T & 3], ((kk * 4) * 5 + (T >> 2)) * 1024 + 5120);
      }
      if (ph < 2) {
        if (moreA) { stageA(2 * ph, t + 1, oth); stageA(2 * ph + 1, t + 1, oth); }
      } else if (moreB) {
        if (ph == 2) { stageB(0, tb, bdst); stageB(1, tb, bdst); stageB(2, tb, bdst); }
        else { stageB(3, tb, bdst); stageB(4, tb, bdst); }
      }
      if (WC == 1) {
        if (ph == 1) { if (moreA) WAIT_V(4); else WAIT_V(0); }
        if (ph == 3) { if (moreB) WAIT_V(5); else WAIT_V(0); }
      }
      asm volatile("s_waitcnt lgkmcnt(0)"
                   : "+v"(bl[0]), "+v"(bh[0]), "+v"(bl[1]), "+v"(bh[1]), "+v"(bl[2]), "+v"(bh[2]), "+v"(bl[3]), "+v"(bh[3]),
                     "+v"(bl[4]), "+v"(bh[4])::"memory");
      SCHED();
      BAR();
      SCHED();
#pragma unroll
      for (int n = 0; n < 5; ++n) bfv[n] = cat8(bl[n], bh[n]);
      __builtin_amdgcn_s_setprio(1);
#pragma unroll
      for (int n = 0; n < 5; ++n) {
        const int T = K1Tiles<WC>::T[nh * 5 + n];
#pragma unroll
        for (int m = 0; m < 4; ++m) {
          if (T >= 2 && T < 6) acc[m][nh * 5 + n] = MFMA(af[m], bfv[n], acc[m][nh * 5 + n]);
          else acc[m][nh * 5 + n] = MFMA(bfv[n], af[m], acc[m][nh * 5 + n]);
        }
      }
      __builtin_amdgcn_s_setprio(0);
      if (WC == 0 && ph == 3) { if (moreB) WAIT_V(5); else WAIT_V(0); }
      SCHED();
      BAR();
      SCHED();
    }
  } while (++t < nt);
  if (WC == 0) BAR();
  ST(0, 2);
  const int row0 = 256 * r + 64 * wm;
  const int grow = 256 * r + 8 * c + w;
  u32x4 gx[2], gw[2];
#pragma unroll
  for (int q = 0; q < 2; ++q) {
    gx[q] = ((const u32x4*)(p.x + (size_t)grow * 2048))[lane * 2 + q];
    gw[q] = ((const u32x4*)p.gate_w)[lane * 2 + q];
  }
  if (WC == 1) {
#pragma unroll
    for (int m = 0; m < 4; ++m) {
      const f32x4 a = acc[m][0], b = acc[m][1];
      u32x4 v = {phi2(a[0], a[1]), phi2(a[2], a[3]), phi2(b[0], b[1]), phi2(b[2], b[3])};
      *(u32x4*)(p.pq + ((size_t)((row0 >> 4) + m) * 32 + c) * 1024 + lane * 16) = v;
    }
  } else {
    f32x4 kv[2][3];
#pragma unroll
    for (int dt = 0; dt < 2; ++dt)
#pragma unroll
      for (int et = 0; et < 3; ++et) kv[dt][et] = (f32x4){0.f, 0.f, 0.f, 0.f};
    const u32x4 ones = {0x3f803f80u, 0x3f803f80u, 0x3f803f80u, 0x3f803f80u};
#pragma unroll
    for (int ks = 0; ks < 2; ++ks) {
      bf16x8 kf[2], vf[3];
#pragma unroll
      for (int dt = 0; dt < 2; ++dt) {
        const f32x4 a = acc[2 * ks][dt], b = acc[2 * ks + 1][dt];
        u32x4 v = {phi2(a[0], a[1]), phi2(a[2], a[3]), phi2(b[0], b[1]), phi2(b[2], b[3])};
        kf[dt] = __builtin_bit_cast(bf16x8, v);
        const f32x4 a2 = acc[2 * ks][2 + dt], b2 = acc[2 * ks + 1][2 + dt];
        u32x4 v2 = {pack2(a2[0], a2[1]), pack2(a2[2], a2[3]), pack2(b2[0], b2[1]), pack2(b2[2], b2[3])};
        vf[dt] = __builtin_bit_cast(bf16x8, v2);
      }
      vf[2] = __builtin_bit_cast(bf16x8, ones);
#pragma unroll
      for (int dt = 0; dt < 2; ++dt)
#pragma unroll
        for (int et = 0; et < 3; ++et) kv[dt][et] = MFMA(kf[dt], vf[et], kv[dt][et]);
    }
#pragma unroll
    for (int dt = 0; dt < 2; ++dt)
#pragma unroll
      for (int et = 0; et < 3; ++et)
        *(__attribute__((address_space(3))) f32x4*)(uintptr_t)(sbase + ((wm * 6 + dt * 3 + et) * 64 + lane) * 16) = kv[dt][et];
  }
  WAIT_L(0);
  BAR();
  asm volatile("" ::: "memory");
  {
    const int tid = w * 64 + lane;
    if (tid < 384) {
      f32x4 s = (f32x4){0.f, 0.f, 0.f, 0.f};
#pragma unroll
      for (int q = 0; q < 4; ++q) s += *(__attribute__((address_space(3))) f32x4*)(uintptr_t)(sbase + (q * 384 + tid) * 16);
      *(f32x4*)(p.kvp + ((size_t)(r * 32 + c) * 384 + tid) * 4) = s;
    }
  }
  {
    constexpr int NP = WC == 0 ? 1 : 2;
#pragma unroll
    for (int pr = 0; pr < NP; ++pr) {
      const int s0 = WC == 0 ? 0 : 2 + 2 * pr;
      const int n0 = WC == 0 ? 4 : 2 + 4 * pr;
      const int j0 = (c & 1) ? (s0 < 2 ? s0 + 5 : s0 - 1) : s0;
      const int J = 7 * c + j0;
#pragma unroll
      for (int m = 0; m < 4; ++m) {
        const f32x4 g0 = acc[m][n0], u0 = acc[m][n0 + 1], g1 = acc[m][n0 + 2], u1 = acc[m][n0 + 3];
        u32x4 v = {mul2(g0[0], g0[1], u0[0], u0[1]), mul2(g0[2], g0[3], u0[2], u0[3]), mul2(g1[0], g1[1], u1[0], u1[1]),
                   mul2(g1[2], g1[3], u1[2], u1[3])};
        *(u32x4*)(p.a2 + ((size_t)(row0 + 16 * m + l15) * KA2 + 16 * J + 8 * g) * 2) = v;
      }
    }
    if (WC == 0) {
      const int J = 7 * c + ((c & 1) ? 0 : 6);
#pragma unroll
      for (int m = 0; m < 4; ++m) {
        const f32x4 g0 = acc[m][8], u0 = acc[m][9];
        u32x2 v = {mul2(g0[0], g0[1], u0[0], u0[1]), mul2(g0[2], g0[3], u0[2], u0[3])};
        *(u32x2*)(p.a2 + ((size_t)(row0 + 16 * m + l15) * KA2 + 32 * (J >> 1) + 8 * g + 4 * (J & 1)) * 2) = v;
      }
    }
  }
  {
    float sg = 0.f;
#pragma unroll
    for (int q = 0; q < 2; ++q)
#pragma unroll
      for (int e = 0; e < 4; ++e) {
        const unsigned pr = pack2(lo_f(gx[q][e]) * lo_f(gw[q][e]), hi_f(gx[q][e]) * hi_f(gw[q][e]));
        sg += lo_f(pr) + hi_f(pr);
      }
#pragma unroll
    for (int o = 32; o > 0; o >>= 1) sg += __shfl_xor(sg, o);
    if (lane == 0) {
      const float gg = bfr(1.0f / (1.0f + __expf(-bfr(sg))));
      p.gbuf[grow] = gg;
      p.gbuf[SEQ + grow] = bfr(1.0f - gg);
    }
  }
  ST(0, 3);
  ST(0, 4); ST(0, 7);
}

__global__ __launch_bounds__(512, 2) void k1_kernel(K1Args p) {
  __shared__ __attribute__((aligned(1024))) char smem[2 * K1_BUF];
  const int lane = threadIdx.x & 63;
  const int w = __builtin_amdgcn_readfirstlane(threadIdx.x >> 6);
  const int bid = blockIdx.x;
  const int xcd = bid & 7, idx = bid >> 3;
  const int r = idx & 7, c = xcd * 4 + (idx >> 3);
  const unsigned sbase = (unsigned)(uintptr_t)(__attribute__((address_space(3))) char*)smem;
  if (bid == 0) p.flags[threadIdx.x] = 0u;
  if (w < 4) k1_body<0>(p, sbase, lane, w, r, c);
  else k1_body<1>(p, sbase, lane, w, r, c);
}

#define K2_RS 1
#define K2_NO 40
constexpr int K2_SUB = 32768, K2_NH = 72 - K2_NO, K2_J0 = K2_NO - 16, K2_G0 = K2_J0 / 4;
static_assert(K2_NO % 4 == 0 && K2_G0 >= 4, "");

#define PIN_ACC()                                                                                                        \
  asm volatile("" : "+v"(acc[0][0]), "+v"(acc[0][1]), "+v"(acc[0][2]), "+v"(acc[0][3]), "+v"(acc[1][0]), "+v"(acc[1][1]), \
                    "+v"(acc[1][2]), "+v"(acc[1][3]), "+v"(acc[2][0]), "+v"(acc[2][1]), "+v"(acc[2][2]), "+v"(acc[2][3]), \
                    "+v"(acc[3][0]), "+v"(acc[3][1]), "+v"(acc[3][2]), "+v"(acc[3][3]))
#define WAIT_FRAG(a, l, h)                                                                                               \
  asm volatile("s_waitcnt lgkmcnt(0)" : "+v"(l[0]), "+v"(h[0]), "+v"(l[1]), "+v"(h[1]), "+v"(l[2]), "+v"(h[2]), "+v"(l[3]), \
               "+v"(h[3]), "+v"(a[0]), "+v"(a[1]), "+v"(a[2]), "+v"(a[3])::"memory")
#define LDS_RD(dst, addr, off) asm volatile("ds_read_b128 %0, %1 offset:%2" : "+v"(dst) : "v"(addr), "i"(off) : "memory")
#define LDS_TR2(dst, addr, off) asm volatile("ds_read_b64_tr_b16 %0, %1 offset:%2" : "+v"(dst) : "v"(addr), "i"(off) : "memory")
#define MFMA_ACC(c, x, y) asm volatile("v_mfma_f32_16x16x32_bf16 %0, %1, %2, %0" : "+v"(c) : "v"(x), "v"(y))
struct K2Args {
  char* a2; const char* wd; const char* wo; const float* gbuf; const char* pq; const float* kvp;
  float* xch; unsigned* flags; char* out;
};

__global__ __launch_bounds__(768, 3) void k2_kernel(K2Args p) {
  __shared__ __attribute__((aligned(1024))) char smem[4 * K2_SUB + 3072 + 4096];
  const int lane = threadIdx.x & 63;
  const int w = __builtin_amdgcn_readfirstlane(threadIdx.x >> 6);
  const int wm = w & 1, wn = (w >> 1) & 1;
  const int bid = blockIdx.x;
  const int xcd = bid & 7, idx = bid >> 3;
  const int s = xcd & 1, qd = xcd >> 1;
  const int tm = (qd >> 1) * 8 + (idx & 7), tn = (qd & 1) * 4 + (idx >> 3);
  const int tile = tm * 8 + tn;
  const int hi = qd * 32 + idx, ro = hi >> 5, hd = hi & 31;
  const unsigned sbase = (unsigned)(uintptr_t)(__attribute__((address_space(3))) char*)smem;
  const unsigned ksm = sbase + 4 * K2_SUB;
  const int g = lane >> 4, l15 = lane & 15, li = lane >> 3, pc = lane & 7;
  const int kb = s ? K2_J0 : 0, ns = s ? K2_NH : K2_NO;
  const int lw = w - 4;
  const char* abase = p.a2 + (size_t)(128 * tm + 16 * lw) * (KA2 * 2);
  const unsigned voffA0 = li * (KA2 * 2) + ((pc ^ (lane >> 4)) * 16), voffA1 = (voffA0 ^ 64) + 8 * KA2 * 2;
  const unsigned voffB = li * 2048 + (((pc >> 1) ^ (li >> 1)) * 32) + (pc & 1) * 16;
  const int boff = tn * 256 + lw * 16384;
  auto stage = [&](int j) {
    const unsigned buf = sbase + (j & 3) * 16384 + lw * 2048;
    if (!s && j >= K2_J0) j += K2_NH;
    j += kb;
    const char* bsrc = (j < 56 ? p.wd + (size_t)j * (64 * 2048) : p.wo + (size_t)(j - 56) * (64 * 2048)) + boff;
    glds_s(abase + j * 128, voffA0, buf);
    glds_s(abase + j * 128, voffA1, buf + 1024);
    glds_s(bsrc, voffB, buf + 65536);
    glds_s(bsrc + 128, voffB, buf + 65536 + 1024);
  };
  ST(1, 0); ST(1, 6);
  if (w >= 4) {
#pragma unroll
    for (int j = 0; j < 4; ++j) stage(j);
    if (s) {
      f32x4 kp[8];
      bf16x8 pf[4];
      float gv[4];
      const int tt = lw < 6 ? lw : 0;
#pragma unroll
      for (int q = 0; q < 8; ++q) kp[q] = *(const f32x4*)(p.kvp + ((size_t)(q * 32 + hd) * 384 + tt * 64 + lane) * 4);
#pragma unroll
      for (int m = 0; m < 4; ++m) {
        pf[m] = *(const bf16x8*)(p.pq + ((size_t)(32 * ro + 4 * lw + m) * 32 + hd) * 1024 + lane * 16);
        gv[m] = p.gbuf[512 * ro + 64 * lw + 16 * m + l15];
      }
      {
        const f32x4 sum = ((kp[0] + kp[1]) + (kp[2] + kp[3])) + ((kp[4] + kp[5]) + (kp[6] + kp[7]));
        const int dt = tt >= 3, et = tt - 3 * dt;
        u32x2 v = {pack2(sum[0], sum[1]), pack2(sum[2], sum[3])};
        if (lw < 6) *(__attribute__((address_space(3))) u32x2*)(uintptr_t)(ksm + (et * 64 + lane) * 16 + dt * 8) = v;
      }
      asm volatile("s_waitcnt vmcnt(0) lgkmcnt(0)" : "+v"(pf[0]), "+v"(pf[1]), "+v"(pf[2]), "+v"(pf[3]), "+v"(gv[0]), "+v"(gv[1]),
                   "+v"(gv[2]), "+v"(gv[3])::"memory");
      BAR();
      asm volatile("" ::: "memory");
      bf16x8 kf[3];
#pragma unroll
      for (int et = 0; et < 3; ++et) kf[et] = lds_b128(ksm + (et * 64 + lane) * 16);
#pragma unroll
      for (int m = 0; m < 4; ++m) {
        const f32x4 z = {0.f, 0.f, 0.f, 0.f};
        f32x4 n0 = MFMA(kf[0], pf[m], z), n1 = MFMA(kf[1], pf[m], z), dn = MFMA(kf[2], pf[m], z);
        const float sc = gv[m] * __builtin_amdgcn_rcpf(dn[0] + 1e-6f);
        n0 *= sc;
        n1 *= sc;
        u32x4 v = {pack2(n0[0], n0[1]), pack2(n0[2], n0[3]), pack2(n1[0], n1[1]), pack2(n1[2], n1[3])};
        char* dst = p.a2 + ((size_t)(512 * ro + 64 * lw + 16 * m + l15) * KA2 + FF + 32 * hd + 8 * g) * 2;
        asm volatile("global_store_dwordx4 %0, %1, off sc1\n\ts_nop 1" ::"v"(dst), "v"(v) : "memory");
      }
    } else {
      WAIT_V(12);
      BAR();
      WAIT_V(8);
    }
    BAR();
    for (int j = 0; j < ns - 1; ++j) {
      if (j + 4 < ns) { stage(j + 4); WAIT_V(8); }
      else if (j + 4 == ns) WAIT_V(4);
      else WAIT_V(0);
      BAR();
    }
    return;
  }
  {
    f32x4 omg;
#pragma unroll
    for (int m = 0; m < 4; ++m) omg[m] = p.gbuf[SEQ + 128 * tm + 64 * wm + 16 * m + l15];
    *(__attribute__((address_space(3))) f32x4*)(uintptr_t)(ksm + 3072 + threadIdx.x * 16) = omg;
  }
  f32x4 acc[4][4];
#pragma unroll
  for (int m = 0; m < 4; ++m)
#pragma unroll
    for (int n = 0; n < 4; ++n) acc[m][n] = (f32x4){0.f, 0.f, 0.f, 0.f};
  unsigned rA[2], rB[4];
  rA[0] = sbase + (64 * wm + l15) * 128 + ((g ^ (l15 >> 1)) * 16);
  rA[1] = rA[0] ^ 64;
  {
    const int i = (g & 1) * 4 + (l15 >> 2), pp = lane & 3;
#pragma unroll
    for (int tp = 0; tp < 4; ++tp)
      rB[tp] = sbase + 65536 + ((g >> 1) * 2 + wn) * 1024 + i * 128 + ((tp ^ (i >> 1)) * 2 + (pp >> 1)) * 16 + (pp & 1) * 8;
  }
  PIN_ACC();
  asm volatile("s_waitcnt vmcnt(0) lgkmcnt(0)" : "+v"(rA[0]), "+v"(rA[1]), "+v"(rB[0]), "+v"(rB[1]), "+v"(rB[2]), "+v"(rB[3])::"memory");
  const int ng = s ? K2_NH / 4 : K2_G0;
  const unsigned endm = (1u << (ng - 1)) | (w == 0 ? (s ? 1u : (7u << (K2_G0 - 4))) : 0u);
  BAR();
  ST(1, 1);
  bf16x8 a0[4] = {}, a1[4] = {};
  s16x4 l0[4] = {}, h0[4] = {}, l1[4] = {}, h1[4] = {};
#pragma unroll
  for (int q = 0; q < 4; ++q) {
    LDS_RD(a0[q], rA[0], q * 2048);
    LDS_TR(l0[q], rB[q], 0);
    LDS_TR(h0[q], rB[q], 4096);
  }
  WAIT_FRAG(a0, l0, h0);
#define K2_READ(r, a, l, h, u, kh)                                                                          \
  do {                                                                                                      \
    if ((r) % 3 == 0) LDS_RD(a[(r) / 3], rA[kh], (u) * 16384 + ((r) / 3) * 2048); \
    else if ((r) % 3 == 1) LDS_TR2(l[(r) / 3], rB[(r) / 3], (u) * 16384 + (kh) * 8192); \
    else LDS_TR2(h[(r) / 3], rB[(r) / 3], (u) * 16384 + (kh) * 8192 + 4096); \
  } while (0)
  auto group = [&]() __attribute__((always_inline)) {
#pragma unroll
    for (int u = 0; u < 4; ++u) {
      SCHED();
#pragma unroll
      for (int t = 0; t < 16; ++t) {
        MFMA_ACC(acc[t & 3][t >> 2], cat8(l0[t >> 2], h0[t >> 2]), a0[t & 3]);
        SCHED();
#pragma unroll
        for (int r = t * K2_RS; r < (t + 1) * K2_RS; ++r)
          if (r < 12) K2_READ(r, a1, l1, h1, u, 1);
        SCHED();
      }
      WAIT_FRAG(a1, l1, h1);
      BAR();
      SCHED();
#pragma unroll
      for (int t = 0; t < 16; ++t) {
        MFMA_ACC(acc[t & 3][t >> 2], cat8(l1[t >> 2], h1[t >> 2]), a1[t & 3]);
        SCHED();
#pragma unroll
        for (int r = t * K2_RS; r < (t + 1) * K2_RS; ++r)
          if (r < 12) K2_READ(r, a0, l0, h0, (u + 1) & 3, 0);
        SCHED();
      }
      WAIT_FRAG(a0, l0, h0);
    }
    SCHED();
  };
  for (int jg = 0;;) {
    group();
    if (!((endm >> jg++) & 1u)) continue;
    if (jg == ng) break;
    if (s) {
      if (threadIdx.x == 0) __hip_atomic_fetch_add((gu32*)(p.flags + ro * 32), 1u, __ATOMIC_RELAXED, __HIP_MEMORY_SCOPE_AGENT);
    } else {
      gu32* f = (gu32*)(p.flags + (tm >> 2) * 32);
      if (jg == K2_G0 - 3) {
        asm volatile("s_mov_b32 m0, %1\n\ts_nop 0\n\tglobal_load_lds_dword %0, off sc1" ::"v"(f), "s"(ksm) : "memory");
      } else if (jg == K2_G0 - 2) {
        WAIT_V(0);
        if (*(volatile __attribute__((address_space(3))) unsigned*)(uintptr_t)ksm < 32u)
          while (__hip_atomic_load(f, __ATOMIC_RELAXED, __HIP_MEMORY_SCOPE_AGENT) < 32u) __builtin_amdgcn_s_sleep(2);
        asm volatile("buffer_inv sc1" ::: "memory");
      } else {
        WAIT_V(0);
      }
    }
  }
  int tid = threadIdx.x;
  asm volatile("s_nop 7\n\ts_nop 7" : "+v"(tid));
  PIN_ACC();
  {
    const f32x4 omg = *(__attribute__((address_space(3))) f32x4*)(uintptr_t)(ksm + 3072 + tid * 16);
#pragma unroll
    for (int m = 0; m < 4; ++m)
#pragma unroll
      for (int n = 0; n < 4; ++n) acc[m][n] *= omg[m];
  }
  if (!s) {
    PIN_ACC();
    asm volatile("s_nop 3");
#pragma nounroll
    for (int jg = 0; jg < 4; ++jg) group();
    asm volatile("s_nop 7\n\ts_nop 7" : "+v"(tid));
    PIN_ACC();
  }
  ST(1, 2);
  {
    const int lane = tid & 63, g = lane >> 4, l15 = lane & 15;
    gu32* f = (gu32*)(p.flags + 256 + tile);
    if (s) {
      float* xw = p.xch + ((size_t)tile * 4 + w) * 4096 + lane * 4;
#pragma unroll
      for (int q = 0; q < 16; ++q)
        asm volatile("global_store_dwordx4 %0, %1, off sc1\n\ts_nop 1" ::"v"(xw + q * 256), "v"(acc[q >> 2][q & 3]) : "memory");
      ST(1, 3);
      WAIT_V(0);
      BAR();
      if (tid == 0) __hip_atomic_store(f, 1u, __ATOMIC_RELAXED, __HIP_MEMORY_SCOPE_AGENT);
    } else {
      while (__hip_atomic_load(f, __ATOMIC_RELAXED, __HIP_MEMORY_SCOPE_AGENT) == 0u) __builtin_amdgcn_s_sleep(1);
      const __amdgpu_buffer_rsrc_t xr =
          __builtin_amdgcn_make_buffer_rsrc((void*)(p.xch + ((size_t)tile * 4 + w) * 4096), 0, 16384, 0x00020000);
      f32x4 o[16];
#pragma unroll
      for (int q = 0; q < 16; ++q)
        o[q] = __builtin_bit_cast(f32x4, __builtin_amdgcn_raw_buffer_load_b128(xr, lane * 16 + q * 1024, 0, 16));
      ST(1, 3);
#pragma unroll
      for (int q = 0; q < 16; ++q) {
        const int m = q >> 2, n = q & 3;
        f32x4 v = acc[m][n] + o[q];
        u32x2 pk = {pack2(v[0], v[1]), pack2(v[2], v[3])};
        const int row = 128 * tm + 64 * wm + 16 * m + l15;
        *(u32x2*)(p.out + ((size_t)row * DM + 128 * tn + 64 * wn + 16 * n + 4 * g) * 2) = pk;
      }
    }
  }
  ST(1, 4); ST(1, 7);
}

extern "C" void kernel_launch(void* const* d_in, const int* in_sizes, int n_in, void* d_out, int out_size, void* d_ws,
                              size_t ws_size, hipStream_t stream) {
  char* ws = (char*)d_ws;
  unsigned* flags = (unsigned*)ws;
  float* gbuf = (float*)(ws + 4096);
  float* kvp = (float*)(ws + 65536);
  char* pq = ws + (2u << 20);
  char* a2 = ws + (8u << 20);
  float* xch = (float*)(ws + (32u << 20));
  K1Args a;
  a.x = (const char*)d_in[0]; a.wqkv = (const char*)d_in[1]; a.wg = (const char*)d_in[4]; a.wu = (const char*)d_in[5];
  a.gate_w = (const char*)d_in[7]; a.a2 = a2; a.pq = pq; a.kvp = kvp; a.gbuf = gbuf; a.flags = flags;
  k1_kernel<<<256, 512, 0, stream>>>(a);
  K2Args b;
  b.a2 = a2; b.wd = (const char*)d_in[6]; b.wo = (const char*)d_in[2]; b.gbuf = gbuf; b.pq = pq; b.kvp = kvp; b.xch = xch; b.flags = flags;
  b.out = (char*)d_out;
  k2_kernel<<<256, 768, 0, stream>>>(b);
}
```

```cpp
#include <hip/hip_runtime.h>
#include <hip/hip_bf16.h>
#include <stdint.h>

typedef __attribute__((ext_vector_type(8))) short bf16x8;
typedef __attribute__((ext_vector_type(4))) short s16x4;
typedef __attribute__((ext_vector_type(4))) float f32x4;
typedef __attribute__((ext_vector_type(4))) unsigned u32x4;
typedef __attribute__((ext_vector_type(2))) unsigned u32x2;
typedef __attribute__((ext_vector_type(2))) float f32x2;
typedef __attribute__((ext_vector_type(2))) __bf16 bf16x2_t;
typedef __attribute__((address_space(3))) s16x4* lds_s16x4_p;
typedef __attribute__((address_space(3))) void* lds_void_p;
typedef __attribute__((address_space(1))) const void* glb_cvoid_p;
typedef __attribute__((address_space(1))) unsigned gu32;

#define DEV __device__ __forceinline__
#define BAR() __builtin_amdgcn_s_barrier()
#define SCHED() __builtin_amdgcn_sched_barrier(0)
#define WAIT_V(n) asm volatile("s_waitcnt vmcnt(" #n ")" ::: "memory")
#define WAIT_L(n) asm volatile("s_waitcnt lgkmcnt(" #n ")" ::: "memory")
#define MFMA(a, b, c) __builtin_amdgcn_mfma_f32_16x16x32_bf16(a, b, c, 0, 0, 0)

#define ST(k, i)
constexpr int SEQ = 2048, DM = 1024, FF = 3584, KA2 = 4608;

DEV unsigned pack2(float a, float b) {
  f32x2 v = {a, b};
  bf16x2_t r = __builtin_convertvector(v, bf16x2_t);
  return __builtin_bit_cast(unsigned, r);
}
DEV float bfr(float a) { return (float)(__bf16)a; }
DEV float lo_f(unsigned u) { return __builtin_bit_cast(float, u << 16); }
DEV float hi_f(unsigned u) { return __builtin_bit_cast(float, u & 0xffff0000u); }
DEV unsigned phi2(float a, float b) {
  const float ya = a > 0.f ? a + 1.0f : __expf(a), yb = b > 0.f ? b + 1.0f : __expf(b);
  return pack2(ya + 1e-6f, yb + 1e-6f);
}
DEV unsigned mul2(float g0, float g1, float u0, float u1) { return pack2(g0 * u0, g1 * u1); }
DEV void glds_s(const char* ubase, unsigned voff, unsigned lds_addr) {
  asm volatile("s_mov_b32 m0, %2\n\ts_nop 0\n\tglobal_load_lds_dwordx4 %0, %1" ::"v"(voff), "s"(ubase), "s"(lds_addr)
               : "memory");
}
DEV void glds_m(const char* ubase, unsigned voff, unsigned lds_addr, int em) {
  asm volatile("s_mov_b32 m0, %2\n\ts_or_b32 exec_lo, %3, 1\n\ts_mov_b32 exec_hi, %3\n\tglobal_load_lds_dwordx4 %0, %1\n\ts_mov_b64 exec, -1" ::"v"(voff), "s"(ubase), "s"(lds_addr), "s"(em)
               : "memory", "scc");
}
DEV void glds_v(const char* vptr, unsigned lds_addr) {
  asm volatile("s_mov_b32 m0, %1\n\ts_nop 0\n\tglobal_load_lds_dwordx4 %0, off" ::"v"(vptr), "s"(lds_addr)
               : "memory");
}
DEV bf16x8 lds_b128(unsigned addr) { return *(__attribute__((address_space(3))) bf16x8*)(uintptr_t)addr; }
#define LDS_TR(dst, addr, off) asm volatile("ds_read_b64_tr_b16 %0, %1 offset:%2" : "=v"(dst) : "v"(addr), "i"(off) : "memory")
DEV bf16x8 cat8(s16x4 a, s16x4 b) { return __builtin_shufflevector(a, b, 0, 1, 2, 3, 4, 5, 6, 7); }

constexpr int K1_A = 32768, K1_BUF = 73728, K1_NT = DM / 64;

template <int WC> struct K1Tiles;
template <> struct K1Tiles<0> { static constexpr int T[10] = {2, 3, 4, 5, 6, 13, 7, 14, 12, 19}; };
template <> struct K1Tiles<1> { static constexpr int T[10] = {0, 1, 8, 15, 9, 16, 10, 17, 11, 18}; };

struct K1Args {
  const char* x; const char* wqkv; const char* wg; const char* wu; const char* gate_w;
  char* a2; char* pq; float* kvp; float* gbuf; unsigned* flags; const char* wd; const char* wo;
};

template <int WC>
DEV void k1_body(const K1Args& p, unsigned sbase, int lane, int w, int r, int c) {
  const int wm = w & 3;
  const int g = lane >> 4, l15 = lane & 15;
  const int li = lane >> 3, pc = lane & 7;
  const char* xbase = p.x + (size_t)(256 * r + 32 * w) * 2048;
  unsigned voffA0 = li * 2048 + ((pc ^ (lane >> 4)) * 16);
  unsigned voffA1 = voffA0 ^ 64;
  const char* pB1; const char* pB3;
  unsigned incB1, voffB[3];
  {
    const int krow = 32 * (w >> 2) + 16 * ((w >> 1) & 1) + 4 * (w & 1) + (li & 3) + 8 * (li >> 2);
    const int tp = (pc >> 1) ^ (li >> 1);
    auto off_qkv = [&](int T) { return (unsigned)(krow * 6144 + ((T >> 1) * 1024 + 32 * c + 16 * (T & 1) + 8 * (pc & 1)) * 2); };
    auto off_gu = [&](int sj) {
      const int j = (c & 1) ? (sj < 2 ? sj + 5 : (sj < 6 ? sj - 1 : 0)) : sj;
      return (unsigned)(krow * 7168 + (112 * c + 16 * j + 8 * (pc & 1)) * 2);
    };
    voffB[0] = off_qkv(tp);
    voffB[1] = off_gu(2 + tp);
    voffB[2] = off_gu(3 + tp);
    pB1 = tp < 2 ? p.wqkv + off_qkv(4 + tp) : p.wg + off_gu(tp - 2);
    incB1 = tp < 2 ? 64 * 6144 : 64 * 7168;
    pB3 = tp < 1 ? p.wg + off_gu(6) : p.wu + off_gu(tp - 1);
  }
  unsigned rA[2], rB[4];
  rA[0] = (64 * wm + l15) * 128 + ((g ^ (l15 >> 1)) * 16);
  rA[1] = rA[0] ^ 64;
  {
    const int i = (g & 1) * 4 + (l15 >> 2), pp = lane & 3;
#pragma unroll
    for (int tp = 0; tp < 4; ++tp)
      rB[tp] = K1_A + (g >> 1) * 10240 + i * 128 + ((tp ^ (i >> 1)) * 2 + (pp >> 1)) * 16 + (pp & 1) * 8;
  }
  auto stageA = [&](int i, int kt, unsigned buf) {
    glds_s(xbase + i * 16384 + kt * 128, (i & 1) ? voffA1 : voffA0, buf + (4 * w + i) * 1024);
  };
  auto stageB = [&](int G, int kt, unsigned buf) {
    const unsigned dst = buf + K1_A + (w * 5 + G) * 1024;
    if (G == 0) glds_s(p.wqkv + (size_t)kt * (64 * 6144), voffB[0], dst);
    if (G == 2) glds_s(p.wg + (size_t)kt * (64 * 7168), voffB[1], dst);
    if (G == 4) glds_s(p.wu + (size_t)kt * (64 * 7168), voffB[2], dst);
    if (G == 1) { glds_v(pB1, dst); pB1 += incB1; }
    if (G == 3) { glds_v(pB3, dst); pB3 += 64 * 7168; }
  };

  f32x4 acc[4][10];
#pragma unroll
  for (int m = 0; m < 4; ++m)
#pragma unroll
    for (int n = 0; n < 10; ++n) acc[m][n] = (f32x4){0.f, 0.f, 0.f, 0.f};

  ST(0, 0); ST(0, 6);
#pragma unroll
  for (int i = 0; i < 4; ++i) stageA(i, 0, sbase);
#pragma unroll
  for (int G = 0; G < 5; ++G) stageB(G, 0, sbase);
  if (WC == 0) {
#pragma unroll
    for (int G = 0; G < 5; ++G) stageB(G, 1, sbase + K1_BUF);
  }
  SCHED();
  const char* pT;
  unsigned incT, dumT = 0;
  {
    const int wid = blockIdx.x * 8 + w;
    const int H = wid * 12 + (lane < 12 ? lane : 0);
    const int H1 = H - 4096, H2 = H1 - 6144, H3 = H2 - 7168;
    pT = H1 < 0 ? p.x + (H >> 1) * 2048 + (H & 1) * 64 : (H2 < 0 ? p.wqkv + H1 * 64 : (H3 < 0 ? p.wg + H2 * 64 : p.wu + H3 * 64));
    incT = H1 < 0 ? 128 : (H2 < 0 ? 64 * 6144 : 64 * 7168);
    pT += 3 * incT;
    if (lane >= 12 && lane < 18) {
      const int sid = wid * 6 + lane - 12, s1 = sid * 13, s2 = (sid - 8823) * 13;
      pT = sid < 8823 ? p.wd + (s1 < 114688 - 13 ? s1 : 114688 - 13) * 64 : p.wo + (s2 < 32768 - 13 ? s2 : 32768 - 13) * 64;
      incT = 64;
    }
  }
  auto touch = [&](bool adv) {
    asm volatile("s_mov_b64 exec, 0x3ffff\n\tglobal_load_dword %0, %1, off\n\ts_mov_b64 exec, -1" : "+v"(dumT) : "v"(pT) : "memory");
    pT += adv ? incT : 0u;
  };
  touch(true);
#pragma unroll
  for (int m = 0; m < 4; ++m)
    asm volatile("" : "+v"(acc[m][0]), "+v"(acc[m][1]), "+v"(acc[m][2]), "+v"(acc[m][3]), "+v"(acc[m][4]), "+v"(acc[m][5]),
                      "+v"(acc[m][6]), "+v"(acc[m][7]), "+v"(acc[m][8]), "+v"(acc[m][9]));
  WAIT_V(6);
  BAR();
  if (WC == 1) BAR();
  ST(0, 1);

  bf16x8 af[4], bfv[5];
  int nt = K1_NT;
  asm volatile("" : "+s"(nt));
  int t = 0;
  do {
    const unsigned cur = sbase + (t & 1) * K1_BUF, oth = sbase + ((t & 1) ^ 1) * K1_BUF;
    const bool moreA = t + 1 < nt;
    const bool moreB = WC == 0 ? (t + 2 < nt) : (t + 1 < nt);
    const unsigned bdst = WC == 0 ? cur : oth;
    const int tb = WC == 0 ? t + 2 : t + 1;
    unsigned rBc[4];
#pragma unroll
    for (int q = 0; q < 4; ++q) rBc[q] = cur + rB[q];
#pragma unroll
    for (int ph = 0; ph < 4; ++ph) {
      const int kk = ph >> 1, nh = ph & 1;
      if (nh == 0) {
#pragma unroll
        for (int m = 0; m < 4; ++m) af[m] = lds_b128(cur + rA[kk] + m * 2048);
      }
      s16x4 bl[5], bh[5];
#pragma unroll
      for (int n = 0; n < 5; ++n) {
        const int T = K1Tiles<WC>::T[nh * 5 + n];
        LDS_TR(bl[n], rBc[T & 3], ((kk * 4) * 5 + (T >> 2)) * 1024);
        LDS_TR(bh[n], rBc[T & 3], ((kk * 4) * 5 + (T >> 2)) * 1024 + 5120);
      }
      if (ph < 2) {
        if (moreA) { stageA(2 * ph, t + 1, oth); stageA(2 * ph + 1, t + 1, oth); }
      } else if (moreB) {
        if (ph == 2) { stageB(0, tb, bdst); stageB(1, tb, bdst); stageB(2, tb, bdst); }
        else { stageB(3, tb, bdst); stageB(4, tb, bdst); touch(t + 5 < K1_NT); }
      }
      if (WC == 1) {
        if (ph == 1) { if (moreA) WAIT_V(5); else WAIT_V(0); }
        if (ph == 3) { if (moreB) WAIT_V(6); else WAIT_V(0); }
      }
      asm volatile("s_waitcnt lgkmcnt(0)"
                   : "+v"(bl[0]), "+v"(bh[0]), "+v"(bl[1]), "+v"(bh[1]), "+v"(bl[2]), "+v"(bh[2]), "+v"(bl[3]), "+v"(bh[3]),
                     "+v"(bl[4]), "+v"(bh[4])::"memory");
      SCHED();
      BAR();
      SCHED();
#pragma unroll
      for (int n = 0; n < 5; ++n) bfv[n] = cat8(bl[n], bh[n]);
      __builtin_amdgcn_s_setprio(1);
#pragma unroll
      for (int n = 0; n < 5; ++n) {
        const int T = K1Tiles<WC>::T[nh * 5 + n];
#pragma unroll
        for (int m = 0; m < 4; ++m) {
          if (T >= 2 && T < 6) acc[m][nh * 5 + n] = MFMA(af[m], bfv[n], acc[m][nh * 5 + n]);
          else acc[m][nh * 5 + n] = MFMA(bfv[n], af[m], acc[m][nh * 5 + n]);
        }
      }
      __builtin_amdgcn_s_setprio(0);
      if (WC == 0 && ph == 3) { if (moreB) WAIT_V(6); else WAIT_V(0); }
      SCHED();
      BAR();
      SCHED();
    }
  } while (++t < nt);
  if (WC == 0) BAR();
  ST(0, 2);
  asm volatile("" ::"v"(dumT));
  const int row0 = 256 * r + 64 * wm;
  const int grow = 256 * r + 8 * c + w;
  u32x4 gx[2], gw[2];
#pragma unroll
  for (int q = 0; q < 2; ++q) {
    gx[q] = ((const u32x4*)(p.x + (size_t)grow * 2048))[lane * 2 + q];
    gw[q] = ((const u32x4*)p.gate_w)[lane * 2 + q];
  }
  if (WC == 1) {
#pragma unroll
    for (int m = 0; m < 4; ++m) {
      const f32x4 a = acc[m][0], b = acc[m][1];
      u32x4 v = {phi2(a[0], a[1]), phi2(a[2], a[3]), phi2(b[0], b[1]), phi2(b[2], b[3])};
      *(u32x4*)(p.pq + ((size_t)((row0 >> 4) + m) * 32 + c) * 1024 + lane * 16) = v;
    }
  } else {
    f32x4 kv[2][3];
#pragma unroll
    for (int dt = 0; dt < 2; ++dt)
#pragma unroll
      for (int et = 0; et < 3; ++et) kv[dt][et] = (f32x4){0.f, 0.f, 0.f, 0.f};
    const u32x4 ones = {0x3f803f80u, 0x3f803f80u, 0x3f803f80u, 0x3f803f80u};
#pragma unroll
    for (int ks = 0; ks < 2; ++ks) {
      bf16x8 kf[2], vf[3];
#pragma unroll
      for (int dt = 0; dt < 2; ++dt) {
        const f32x4 a = acc[2 * ks][dt], b = acc[2 * ks + 1][dt];
        u32x4 v = {phi2(a[0], a[1]), phi2(a[2], a[3]), phi2(b[0], b[1]), phi2(b[2], b[3])};
        kf[dt] = __builtin_bit_cast(bf16x8, v);
        const f32x4 a2 = acc[2 * ks][2 + dt], b2 = acc[2 * ks + 1][2 + dt];
        u32x4 v2 = {pack2(a2[0], a2[1]), pack2(a2[2], a2[3]), pack2(b2[0], b2[1]), pack2(b2[2], b2[3])};
        vf[dt] = __builtin_bit_cast(bf16x8, v2);
      }
      vf[2] = __builtin_bit_cast(bf16x8, ones);
#pragma unroll
      for (int dt = 0; dt < 2; ++dt)
#pragma unroll
        for (int et = 0; et < 3; ++et) kv[dt][et] = MFMA(kf[dt], vf[et], kv[dt][et]);
    }
#pragma unroll
    for (int dt = 0; dt < 2; ++dt)
#pragma unroll
      for (int et = 0; et < 3; ++et)
        *(__attribute__((address_space(3))) f32x4*)(uintptr_t)(sbase + ((wm * 6 + dt * 3 + et) * 64 + lane) * 16) = kv[dt][et];
  }
  WAIT_L(0);
  BAR();
  asm volatile("" ::: "memory");
  {
    const int tid = w * 64 + lane;
    if (tid < 384) {
      f32x4 s = (f32x4){0.f, 0.f, 0.f, 0.f};
#pragma unroll
      for (int q = 0; q < 4; ++q) s += *(__attribute__((address_space(3))) f32x4*)(uintptr_t)(sbase + (q * 384 + tid) * 16);
      *(f32x4*)(p.kvp + ((size_t)(r * 32 + c) * 384 + tid) * 4) = s;
    }
  }
  {
    constexpr int NP = WC == 0 ? 1 : 2;
#pragma unroll
    for (int pr = 0; pr < NP; ++pr) {
      const int s0 = WC == 0 ? 0 : 2 + 2 * pr;
      const int n0 = WC == 0 ? 4 : 2 + 4 * pr;
      const int j0 = (c & 1) ? (s0 < 2 ? s0 + 5 : s0 - 1) : s0;
      const int J = 7 * c + j0;
#pragma unroll
      for (int m = 0; m < 4; ++m) {
        const f32x4 g0 = acc[m][n0], u0 = acc[m][n0 + 1], g1 = acc[m][n0 + 2], u1 = acc[m][n0 + 3];
        u32x4 v = {mul2(g0[0], g0[1], u0[0], u0[1]), mul2(g0[2], g0[3], u0[2], u0[3]), mul2(g1[0], g1[1], u1[0], u1[1]),
                   mul2(g1[2], g1[3], u1[2], u1[3])};
        *(u32x4*)(p.a2 + ((size_t)(row0 + 16 * m + l15) * KA2 + 16 * J + 8 * g) * 2) = v;
      }
    }
    if (WC == 0) {
      const int J = 7 * c + ((c & 1) ? 0 : 6);
#pragma unroll
      for (int m = 0; m < 4; ++m) {
        const f32x4 g0 = acc[m][8], u0 = acc[m][9];
        u32x2 v = {mul2(g0[0], g0[1], u0[0], u0[1]), mul2(g0[2], g0[3], u0[2], u0[3])};
        *(u32x2*)(p.a2 + ((size_t)(row0 + 16 * m + l15) * KA2 + 32 * (J >> 1) + 8 * g + 4 * (J & 1)) * 2) = v;
      }
    }
  }
  {
    float sg = 0.f;
#pragma unroll
    for (int q = 0; q < 2; ++q)
#pragma unroll
      for (int e = 0; e < 4; ++e) {
        const unsigned pr = pack2(lo_f(gx[q][e]) * lo_f(gw[q][e]), hi_f(gx[q][e]) * hi_f(gw[q][e]));
        sg += lo_f(pr) + hi_f(pr);
      }
#pragma unroll
    for (int o = 32; o > 0; o >>= 1) sg += __shfl_xor(sg, o);
    if (lane == 0) {
      const float gg = bfr(1.0f / (1.0f + __expf(-bfr(sg))));
      p.gbuf[grow] = gg;
      p.gbuf[SEQ + grow] = bfr(1.0f - gg);
    }
  }
  ST(0, 3);
  ST(0, 4); ST(0, 7);
}

__global__ __launch_bounds__(512, 2) void k1_kernel(K1Args p) {
  __shared__ __attribute__((aligned(1024))) char smem[2 * K1_BUF];
  const int lane = threadIdx.x & 63;
  const int w = __builtin_amdgcn_readfirstlane(threadIdx.x >> 6);
  const int bid = blockIdx.x;
  const int xcd = bid & 7, idx = bid >> 3;
  const int r = idx & 7, c = xcd * 4 + (idx >> 3);
  const unsigned sbase = (unsigned)(uintptr_t)(__attribute__((address_space(3))) char*)smem;
  if (bid == 0) p.flags[threadIdx.x] = 0u;
  if (w < 4) k1_body<0>(p, sbase, lane, w, r, c);
  else k1_body<1>(p, sbase, lane, w, r, c);
}

#define K2_RS 1
#define K2_NO 40
constexpr int K2_SUB = 32768, K2_NH = 72 - K2_NO, K2_J0 = K2_NO - 16, K2_G0 = K2_J0 / 4;
static_assert(K2_NO % 4 == 0 && K2_G0 >= 4, "");

#define PIN_ACC()                                                                                                        \
  asm volatile("" : "+v"(acc[0][0]), "+v"(acc[0][1]), "+v"(acc[0][2]), "+v"(acc[0][3]), "+v"(acc[1][0]), "+v"(acc[1][1]), \
                    "+v"(acc[1][2]), "+v"(acc[1][3]), "+v"(acc[2][0]), "+v"(acc[2][1]), "+v"(acc[2][2]), "+v"(acc[2][3]), \
                    "+v"(acc[3][0]), "+v"(acc[3][1]), "+v"(acc[3][2]), "+v"(acc[3][3]))
#define WAIT_FRAG(a, l, h)                                                                                               \
  asm volatile("s_waitcnt lgkmcnt(0)" : "+v"(l[0]), "+v"(h[0]), "+v"(l[1]), "+v"(h[1]), "+v"(l[2]), "+v"(h[2]), "+v"(l[3]), \
               "+v"(h[3]), "+v"(a[0]), "+v"(a[1]), "+v"(a[2]), "+v"(a[3])::"memory")
#define LDS_RD(dst, addr, off) asm volatile("ds_read_b128 %0, %1 offset:%2" : "+v"(dst) : "v"(addr), "i"(off) : "memory")
#define LDS_TR2(dst, addr, off) asm volatile("ds_read_b64_tr_b16 %0, %1 offset:%2" : "+v"(dst) : "v"(addr), "i"(off) : "memory")
#define MFMA_ACC(c, x, y) asm volatile("v_mfma_f32_16x16x32_bf16 %0, %1, %2, %0" : "+v"(c) : "v"(x), "v"(y))
struct K2Args {
  char* a2; const char* wd; const char* wo; const float* gbuf; const char* pq; const float* kvp;
  float* xch; unsigned* flags; char* out;
};

__global__ __launch_bounds__(768, 3) void k2_kernel(K2Args p) {
  __shared__ __attribute__((aligned(1024))) char smem[4 * K2_SUB + 3072 + 4096];
  const int lane = threadIdx.x & 63;
  const int w = __builtin_amdgcn_readfirstlane(threadIdx.x >> 6);
  const int wm = w & 1, wn = (w >> 1) & 1;
  const int bid = blockIdx.x;
  const int xcd = bid & 7, idx = bid >> 3;
  const int s = xcd & 1, qd = xcd >> 1;
  const int tm = (qd >> 1) * 8 + (idx & 7), tn = (qd & 1) * 4 + (idx >> 3);
  const int tile = tm * 8 + tn;
  const int hi = qd * 32 + idx, ro = hi >> 5, hd = hi & 31;
  const unsigned sbase = (unsigned)(uintptr_t)(__attribute__((address_space(3))) char*)smem;
  const unsigned ksm = sbase + 4 * K2_SUB;
  const int g = lane >> 4, l15 = lane & 15, li = lane >> 3, pc = lane & 7;
  const int kb = s ? K2_J0 : 0, ns = s ? K2_NH : K2_NO;
  const int lw = w - 4;
  const char* abase = p.a2 + (size_t)(128 * tm + 16 * lw) * (KA2 * 2);
  const unsigned voffA0 = li * (KA2 * 2) + ((pc ^ (lane >> 4)) * 16), voffA1 = (voffA0 ^ 64) + 8 * KA2 * 2;
  const unsigned voffB = li * 2048 + (((pc >> 1) ^ (li >> 1)) * 32) + (pc & 1) * 16;
  const int boff = tn * 256 + lw * 16384;
  auto stage = [&](int j) {
    const unsigned buf = sbase + (j & 3) * 16384 + lw * 2048;
    if (!s && j >= K2_J0) j += K2_NH;
    j += kb;
    const char* bsrc = (j < 56 ? p.wd + (size_t)j * (64 * 2048) : p.wo + (size_t)(j - 56) * (64 * 2048)) + boff;
    glds_s(abase + j * 128, voffA0, buf);
    glds_s(abase + j * 128, voffA1, buf + 1024);
    glds_s(bsrc, voffB, buf + 65536);
    glds_s(bsrc + 128, voffB, buf + 65536 + 1024);
  };
  ST(1, 0); ST(1, 6);
  if (w >= 4) {
#pragma unroll
    for (int j = 0; j < 4; ++j) stage(j);
    if (s) {
      f32x4 kp[8];
      bf16x8 pf[4];
      float gv[4];
      const int tt = lw < 6 ? lw : 0;
#pragma unroll
      for (int q = 0; q < 8; ++q) kp[q] = *(const f32x4*)(p.kvp + ((size_t)(q * 32 + hd) * 384 + tt * 64 + lane) * 4);
#pragma unroll
      for (int m = 0; m < 4; ++m) {
        pf[m] = *(const bf16x8*)(p.pq + ((size_t)(32 * ro + 4 * lw + m) * 32 + hd) * 1024 + lane * 16);
        gv[m] = p.gbuf[512 * ro + 64 * lw + 16 * m + l15];
      }
      {
        const f32x4 sum = ((kp[0] + kp[1]) + (kp[2] + kp[3])) + ((kp[4] + kp[5]) + (kp[6] + kp[7]));
        const int dt = tt >= 3, et = tt - 3 * dt;
        u32x2 v = {pack2(sum[0], sum[1]), pack2(sum[2], sum[3])};
        if (lw < 6) *(__attribute__((address_space(3))) u32x2*)(uintptr_t)(ksm + (et * 64 + lane) * 16 + dt * 8) = v;
      }
      asm volatile("s_waitcnt vmcnt(0) lgkmcnt(0)" : "+v"(pf[0]), "+v"(pf[1]), "+v"(pf[2]), "+v"(pf[3]), "+v"(gv[0]), "+v"(gv[1]),
                   "+v"(gv[2]), "+v"(gv[3])::"memory");
      BAR();
      asm volatile("" ::: "memory");
      bf16x8 kf[3];
#pragma unroll
      for (int et = 0; et < 3; ++et) kf[et] = lds_b128(ksm + (et * 64 + lane) * 16);
#pragma unroll
      for (int m = 0; m < 4; ++m) {
        const f32x4 z = {0.f, 0.f, 0.f, 0.f};
        f32x4 n0 = MFMA(kf[0], pf[m], z), n1 = MFMA(kf[1], pf[m], z), dn = MFMA(kf[2], pf[m], z);
        const float sc = gv[m] * __builtin_amdgcn_rcpf(dn[0] + 1e-6f);
        n0 *= sc;
        n1 *= sc;
        u32x4 v = {pack2(n0[0], n0[1]), pack2(n0[2], n0[3]), pack2(n1[0], n1[1]), pack2(n1[2], n1[3])};
        char* dst = p.a2 + ((size_t)(512 * ro + 64 * lw + 16 * m + l15) * KA2 + FF + 32 * hd + 8 * g) * 2;
        asm volatile("global_store_dwordx4 %0, %1, off sc1\n\ts_nop 1" ::"v"(dst), "v"(v) : "memory");
      }
    } else {
      WAIT_V(12);
      BAR();
      WAIT_V(8);
    }
    BAR();
    for (int j = 0; j < ns - 1; ++j) {
      if (j + 4 < ns) { stage(j + 4); WAIT_V(8); }
      else if (j + 4 == ns) WAIT_V(4);
      else WAIT_V(0);
      BAR();
    }
    return;
  }
  {
    f32x4 omg;
#pragma unroll
    for (int m = 0; m < 4; ++m) omg[m] = p.gbuf[SEQ + 128 * tm + 64 * wm + 16 * m + l15];
    *(__attribute__((address_space(3))) f32x4*)(uintptr_t)(ksm + 3072 + threadIdx.x * 16) = omg;
  }
  f32x4 acc[4][4];
#pragma unroll
  for (int m = 0; m < 4; ++m)
#pragma unroll
    for (int n = 0; n < 4; ++n) acc[m][n] = (f32x4){0.f, 0.f, 0.f, 0.f};
  unsigned rA[2], rB[4];
  rA[0] = sbase + (64 * wm + l15) * 128 + ((g ^ (l15 >> 1)) * 16);
  rA[1] = rA[0] ^ 64;
  {
    const int i = (g & 1) * 4 + (l15 >> 2), pp = lane & 3;
#pragma unroll
    for (int tp = 0; tp < 4; ++tp)
      rB[tp] = sbase + 65536 + ((g >> 1) * 2 + wn) * 1024 + i * 128 + ((tp ^ (i >> 1)) * 2 + (pp >> 1)) * 16 + (pp & 1) * 8;
  }
  PIN_ACC();
  asm volatile("s_waitcnt vmcnt(0) lgkmcnt(0)" : "+v"(rA[0]), "+v"(rA[1]), "+v"(rB[0]), "+v"(rB[1]), "+v"(rB[2]), "+v"(rB[3])::"memory");
  const int ng = s ? K2_NH / 4 : K2_G0;
  const unsigned endm = (1u << (ng - 1)) | (w == 0 ? (s ? 1u : (7u << (K2_G0 - 4))) : 0u);
  BAR();
  ST(1, 1);
  bf16x8 a0[4] = {}, a1[4] = {};
  s16x4 l0[4] = {}, h0[4] = {}, l1[4] = {}, h1[4] = {};
#pragma unroll
  for (int q = 0; q < 4; ++q) {
    LDS_RD(a0[q], rA[0], q * 2048);
    LDS_TR(l0[q], rB[q], 0);
    LDS_TR(h0[q], rB[q], 4096);
  }
  WAIT_FRAG(a0, l0, h0);
#define K2_READ(r, a, l, h, u, kh)                                                                          \
  do {                                                                                                      \
    if ((r) % 3 == 0) LDS_RD(a[(r) / 3], rA[kh], (u) * 16384 + ((r) / 3) * 2048); \
    else if ((r) % 3 == 1) LDS_TR2(l[(r) / 3], rB[(r) / 3], (u) * 16384 + (kh) * 8192); \
    else LDS_TR2(h[(r) / 3], rB[(r) / 3], (u) * 16384 + (kh) * 8192 + 4096); \
  } while (0)
  auto group = [&]() __attribute__((always_inline)) {
#pragma unroll
    for (int u = 0; u < 4; ++u) {
      SCHED();
#pragma unroll
      for (int t = 0; t < 16; ++t) {
        MFMA_ACC(acc[t & 3][t >> 2], cat8(l0[t >> 2], h0[t >> 2]), a0[t & 3]);
        SCHED();
#pragma unroll
        for (int r = t * K2_RS; r < (t + 1) * K2_RS; ++r)
          if (r < 12) K2_READ(r, a1, l1, h1, u, 1);
        SCHED();
      }
      WAIT_FRAG(a1, l1, h1);
      BAR();
      SCHED();
#pragma unroll
      for (int t = 0; t < 16; ++t) {
        MFMA_ACC(acc[t & 3][t >> 2], cat8(l1[t >> 2], h1[t >> 2]), a1[t & 3]);
        SCHED();
#pragma unroll
        for (int r = t * K2_RS; r < (t + 1) * K2_RS; ++r)
          if (r < 12) K2_READ(r, a0, l0, h0, (u + 1) & 3, 0);
        SCHED();
      }
      WAIT_FRAG(a0, l0, h0);
    }
    SCHED();
  };
  for (int jg = 0;;) {
    group();
    if (!((endm >> jg++) & 1u)) continue;
    if (jg == ng) break;
    if (s) {
      if (threadIdx.x == 0) __hip_atomic_fetch_add((gu32*)(p.flags + ro * 32), 1u, __ATOMIC_RELAXED, __HIP_MEMORY_SCOPE_AGENT);
    } else {
      gu32* f = (gu32*)(p.flags + (tm >> 2) * 32);
      if (jg == K2_G0 - 3) {
        asm volatile("s_mov_b32 m0, %1\n\ts_nop 0\n\tglobal_load_lds_dword %0, off sc1" ::"v"(f), "s"(ksm) : "memory");
      } else if (jg == K2_G0 - 2) {
        WAIT_V(0);
        if (*(volatile __attribute__((address_space(3))) unsigned*)(uintptr_t)ksm < 32u)
          while (__hip_atomic_load(f, __ATOMIC_RELAXED, __HIP_MEMORY_SCOPE_AGENT) < 32u) __builtin_amdgcn_s_sleep(2);
        asm volatile("buffer_inv sc1" ::: "memory");
      } else {
        WAIT_V(0);
      }
    }
  }
  int tid = threadIdx.x;
  asm volatile("s_nop 7\n\ts_nop 7" : "+v"(tid));
  PIN_ACC();
  {
    const f32x4 omg = *(__attribute__((address_space(3))) f32x4*)(uintptr_t)(ksm + 3072 + tid * 16);
#pragma unroll
    for (int m = 0; m < 4; ++m)
#pragma unroll
      for (int n = 0; n < 4; ++n) acc[m][n] *= omg[m];
  }
  if (!s) {
    PIN_ACC();
    asm volatile("s_nop 3");
#pragma nounroll
    for (int jg = 0; jg < 4; ++jg) group();
    asm volatile("s_nop 7\n\ts_nop 7" : "+v"(tid));
    PIN_ACC();
  }
  ST(1, 2);
  {
    const int lane = tid & 63, g = lane >> 4, l15 = lane & 15;
    gu32* f = (gu32*)(p.flags + 256 + tile);
    if (s) {
      float* xw = p.xch + ((size_t)tile * 4 + w) * 4096 + lane * 4;
#pragma unroll
      for (int q = 0; q < 16; ++q)
        asm volatile("global_store_dwordx4 %0, %1, off sc1\n\ts_nop 1" ::"v"(xw + q * 256), "v"(acc[q >> 2][q & 3]) : "memory");
      ST(1, 3);
      WAIT_V(0);
      BAR();
      if (tid == 0) __hip_atomic_store(f, 1u, __ATOMIC_RELAXED, __HIP_MEMORY_SCOPE_AGENT);
    } else {
      while (__hip_atomic_load(f, __ATOMIC_RELAXED, __HIP_MEMORY_SCOPE_AGENT) == 0u) __builtin_amdgcn_s_sleep(1);
      const __amdgpu_buffer_rsrc_t xr =
          __builtin_amdgcn_make_buffer_rsrc((void*)(p.xch + ((size_t)tile * 4 + w) * 4096), 0, 16384, 0x00020000);
      f32x4 o[16];
#pragma unroll
      for (int q = 0; q < 16; ++q)
        o[q] = __builtin_bit_cast(f32x4, __builtin_amdgcn_raw_buffer_load_b128(xr, lane * 16 + q * 1024, 0, 16));
      ST(1, 3);
#pragma unroll
      for (int q = 0; q < 8; ++q) {
        const int m = q >> 1, n = (q & 1) * 2;
        const f32x4 x = acc[m][n] + o[2 * q], y = acc[m][n + 1] + o[2 * q + 1];
        const auto r0 = __builtin_amdgcn_permlane16_swap(pack2(x[0], x[1]), pack2(y[0], y[1]), false, false);
        const auto r1 = __builtin_amdgcn_permlane16_swap(pack2(x[2], x[3]), pack2(y[2], y[3]), false, false);
        const u32x4 pk = {r0[0], r1[0], r0[1], r1[1]};
        const int row = 128 * tm + 64 * wm + 16 * m + l15;
        *(u32x4*)(p.out + ((size_t)row * DM + 128 * tn + 64 * wn + 16 * n + 16 * (g & 1) + 8 * (g >> 1)) * 2) = pk;
      }
    }
  }
  ST(1, 4); ST(1, 7);
}

extern "C" void kernel_launch(void* const* d_in, const int* in_sizes, int n_in, void* d_out, int out_size, void* d_ws,
                              size_t ws_size, hipStream_t stream) {
  char* ws = (char*)d_ws;
  unsigned* flags = (unsigned*)ws;
  float* gbuf = (float*)(ws + 4096);
  float* kvp = (float*)(ws + 65536);
  char* pq = ws + (2u << 20);
  char* a2 = ws + (8u << 20);
  float* xch = (float*)(ws + (32u << 20));
  K1Args a;
  a.x = (const char*)d_in[0]; a.wqkv = (const char*)d_in[1]; a.wg = (const char*)d_in[4]; a.wu = (const char*)d_in[5];
  a.gate_w = (const char*)d_in[7]; a.a2 = a2; a.pq = pq; a.kvp = kvp; a.gbuf = gbuf; a.flags = flags;
  a.wd = (const char*)d_in[6]; a.wo = (const char*)d_in[2];
  k1_kernel<<<256, 512, 0, stream>>>(a);
  K2Args b;
  b.a2 = a2; b.wd = (const char*)d_in[6]; b.wo = (const char*)d_in[2]; b.gbuf = gbuf; b.pq = pq; b.kvp = kvp; b.xch = xch; b.flags = flags;
  b.out = (char*)d_out;
  k2_kernel<<<256, 768, 0, stream>>>(b);
}
```

```cpp
#include <hip/hip_runtime.h>
#include <hip/hip_bf16.h>
#include <stdint.h>

typedef __attribute__((ext_vector_type(8))) short bf16x8;
typedef __attribute__((ext_vector_type(4))) short s16x4;
typedef __attribute__((ext_vector_type(4))) float f32x4;
typedef __attribute__((ext_vector_type(4))) unsigned u32x4;
typedef __attribute__((ext_vector_type(2))) unsigned u32x2;
typedef __attribute__((ext_vector_type(2))) float f32x2;
typedef __attribute__((ext_vector_type(2))) __bf16 bf16x2_t;
typedef __attribute__((address_space(3))) s16x4* lds_s16x4_p;
typedef __attribute__((address_space(3))) void* lds_void_p;
typedef __attribute__((address_space(1))) const void* glb_cvoid_p;
typedef __attribute__((address_space(1))) unsigned gu32;

#define DEV __device__ __forceinline__
#define BAR() __builtin_amdgcn_s_barrier()
#define SCHED() __builtin_amdgcn_sched_barrier(0)
#define WAIT_V(n) asm volatile("s_waitcnt vmcnt(" #n ")" ::: "memory")
#define WAIT_L(n) asm volatile("s_waitcnt lgkmcnt(" #n ")" ::: "memory")
#define MFMA(a, b, c) __builtin_amdgcn_mfma_f32_16x16x32_bf16(a, b, c, 0, 0, 0)

#define ST(k, i)
constexpr int SEQ = 2048, DM = 1024, FF = 3584, KA2 = 4608;

DEV unsigned pack2(float a, float b) {
  f32x2 v = {a, b};
  bf16x2_t r = __builtin_convertvector(v, bf16x2_t);
  return __builtin_bit_cast(unsigned, r);
}
DEV float bfr(float a) { return (float)(__bf16)a; }
DEV float lo_f(unsigned u) { return __builtin_bit_cast(float, u << 16); }
DEV float hi_f(unsigned u) { return __builtin_bit_cast(float, u & 0xffff0000u); }
DEV unsigned phi2(float a, float b) {
  const float ya = a > 0.f ? a + 1.0f : __expf(a), yb = b > 0.f ? b + 1.0f : __expf(b);
  return pack2(ya + 1e-6f, yb + 1e-6f);
}
DEV unsigned mul2(float g0, float g1, float u0, float u1) { return pack2(g0 * u0, g1 * u1); }
DEV void glds_s(const char* ubase, unsigned voff, unsigned lds_addr) {
  asm volatile("s_mov_b32 m0, %2\n\ts_nop 0\n\tglobal_load_lds_dwordx4 %0, %1" ::"v"(voff), "s"(ubase), "s"(lds_addr)
               : "memory");
}
DEV void glds_m(const char* ubase, unsigned voff, unsigned lds_addr, int em) {
  asm volatile("s_mov_b32 m0, %2\n\ts_or_b32 exec_lo, %3, 1\n\ts_mov_b32 exec_hi, %3\n\tglobal_load_lds_dwordx4 %0, %1\n\ts_mov_b64 exec, -1" ::"v"(voff), "s"(ubase), "s"(lds_addr), "s"(em)
               : "memory", "scc");
}
DEV void glds_v(const char* vptr, unsigned lds_addr) {
  asm volatile("s_mov_b32 m0, %1\n\ts_nop 0\n\tglobal_load_lds_dwordx4 %0, off" ::"v"(vptr), "s"(lds_addr)
               : "memory");
}
DEV bf16x8 lds_b128(unsigned addr) { return *(__attribute__((address_space(3))) bf16x8*)(uintptr_t)addr; }
#define LDS_TR(dst, addr, off) asm volatile("ds_read_b64_tr_b16 %0, %1 offset:%2" : "=v"(dst) : "v"(addr), "i"(off) : "memory")
DEV bf16x8 cat8(s16x4 a, s16x4 b) { return __builtin_shufflevector(a, b, 0, 1, 2, 3, 4, 5, 6, 7); }

constexpr int K1_A = 32768, K1_BUF = 73728, K1_NT = DM / 64;

template <int WC> struct K1Tiles;
template <> struct K1Tiles<0> { static constexpr int T[10] = {2, 3, 4, 5, 6, 13, 7, 14, 12, 19}; };
template <> struct K1Tiles<1> { static constexpr int T[10] = {0, 1, 8, 15, 9, 16, 10, 17, 11, 18}; };

struct K1Args {
  const char* x; const char* wqkv; const char* wg; const char* wu; const char* gate_w;
  char* a2; char* pq; float* kvp; float* gbuf; unsigned* flags; const char* wd; const char* wo;
};

template <int WC>
DEV void k1_body(const K1Args& p, unsigned sbase, int lane, int w, int r, int c) {
  const int wm = w & 3;
  const int g = lane >> 4, l15 = lane & 15;
  const int li = lane >> 3, pc = lane & 7;
  const char* xbase = p.x + (size_t)(256 * r + 32 * w) * 2048;
  unsigned voffA0 = li * 2048 + ((pc ^ (lane >> 4)) * 16);
  unsigned voffA1 = voffA0 ^ 64;
  const char* pB1; const char* pB3;
  unsigned incB1, voffB[3];
  {
    const int krow = 32 * (w >> 2) + 16 * ((w >> 1) & 1) + 4 * (w & 1) + (li & 3) + 8 * (li >> 2);
    const int tp = (pc >> 1) ^ (li >> 1);
    auto off_qkv = [&](int T) { return (unsigned)(krow * 6144 + ((T >> 1) * 1024 + 32 * c + 16 * (T & 1) + 8 * (pc & 1)) * 2); };
    auto off_gu = [&](int sj) {
      const int j = (c & 1) ? (sj < 2 ? sj + 5 : (sj < 6 ? sj - 1 : 0)) : sj;
      return (unsigned)(krow * 7168 + (112 * c + 16 * j + 8 * (pc & 1)) * 2);
    };
    voffB[0] = off_qkv(tp);
    voffB[1] = off_gu(2 + tp);
    voffB[2] = off_gu(3 + tp);
    pB1 = tp < 2 ? p.wqkv + off_qkv(4 + tp) : p.wg + off_gu(tp - 2);
    incB1 = tp < 2 ? 64 * 6144 : 64 * 7168;
    pB3 = tp < 1 ? p.wg + off_gu(6) : p.wu + off_gu(tp - 1);
  }
  unsigned rA[2], rB[4];
  rA[0] = (64 * wm + l15) * 128 + ((g ^ (l15 >> 1)) * 16);
  rA[1] = rA[0] ^ 64;
  {
    const int i = (g & 1) * 4 + (l15 >> 2), pp = lane & 3;
#pragma unroll
    for (int tp = 0; tp < 4; ++tp)
      rB[tp] = K1_A + (g >> 1) * 10240 + i * 128 + ((tp ^ (i >> 1)) * 2 + (pp >> 1)) * 16 + (pp & 1) * 8;
  }
  auto stageA = [&](int i, int kt, unsigned buf) {
    glds_s(xbase + i * 16384 + kt * 128, (i & 1) ? voffA1 : voffA0, buf + (4 * w + i) * 1024);
  };
  auto stageB = [&](int G, int kt, unsigned buf) {
    const unsigned dst = buf + K1_A + (w * 5 + G) * 1024;
    if (G == 0) glds_s(p.wqkv + (size_t)kt * (64 * 6144), voffB[0], dst);
    if (G == 2) glds_s(p.wg + (size_t)kt * (64 * 7168), voffB[1], dst);
    if (G == 4) glds_s(p.wu + (size_t)kt * (64 * 7168), voffB[2], dst);
    if (G == 1) { glds_v(pB1, dst); pB1 += incB1; }
    if (G == 3) { glds_v(pB3, dst); pB3 += 64 * 7168; }
  };

  f32x4 acc[4][10];
#pragma unroll
  for (int m = 0; m < 4; ++m)
#pragma unroll
    for (int n = 0; n < 10; ++n) acc[m][n] = (f32x4){0.f, 0.f, 0.f, 0.f};

  ST(0, 0); ST(0, 6);
#pragma unroll
  for (int i = 0; i < 4; ++i) stageA(i, 0, sbase);
#pragma unroll
  for (int G = 0; G < 5; ++G) stageB(G, 0, sbase);
  if (WC == 0) {
#pragma unroll
    for (int G = 0; G < 5; ++G) stageB(G, 1, sbase + K1_BUF);
  }
  SCHED();
  const char* pT;
  unsigned incT, dumT = 0;
  {
    const int wid = blockIdx.x * 8 + w;
    const int H = wid * 12 + (lane < 12 ? lane : 0);
    const int H1 = H - 4096, H2 = H1 - 6144, H3 = H2 - 7168;
    pT = H1 < 0 ? p.x + (H >> 1) * 2048 + (H & 1) * 64 : (H2 < 0 ? p.wqkv + H1 * 64 : (H3 < 0 ? p.wg + H2 * 64 : p.wu + H3 * 64));
    incT = H1 < 0 ? 128 : (H2 < 0 ? 64 * 6144 : 64 * 7168);
    pT += 3 * incT;
    if (lane >= 12 && lane < 18) {
      const int sid = wid * 6 + lane - 12, s1 = sid * 13, s2 = (sid - 8823) * 13;
      pT = sid < 8823 ? p.wd + (s1 < 114688 - 13 ? s1 : 114688 - 13) * 64 : p.wo + (s2 < 32768 - 13 ? s2 : 32768 - 13) * 64;
      incT = 64;
    }
  }
  auto touch = [&](bool adv) {
    asm volatile("s_mov_b64 exec, 0x3ffff\n\tglobal_load_dword %0, %1, off\n\ts_mov_b64 exec, -1" : "+v"(dumT) : "v"(pT) : "memory");
    pT += adv ? incT : 0u;
  };
  touch(true);
#pragma unroll
  for (int m = 0; m < 4; ++m)
    asm volatile("" : "+v"(acc[m][0]), "+v"(acc[m][1]), "+v"(acc[m][2]), "+v"(acc[m][3]), "+v"(acc[m][4]), "+v"(acc[m][5]),
                      "+v"(acc[m][6]), "+v"(acc[m][7]), "+v"(acc[m][8]), "+v"(acc[m][9]));
  WAIT_V(6);
  BAR();
  if (WC == 1) BAR();
  ST(0, 1);

  bf16x8 af[4], bfv[5];
  int nt = K1_NT;
  asm volatile("" : "+s"(nt));
  int t = 0;
  do {
    const unsigned cur = sbase + (t & 1) * K1_BUF, oth = sbase + ((t & 1) ^ 1) * K1_BUF;
    const bool moreA = t + 1 < nt;
    const bool moreB = WC == 0 ? (t + 2 < nt) : (t + 1 < nt);
    const unsigned bdst = WC == 0 ? cur : oth;
    const int tb = WC == 0 ? t + 2 : t + 1;
    unsigned rBc[4];
#pragma unroll
    for (int q = 0; q < 4; ++q) rBc[q] = cur + rB[q];
#pragma unroll
    for (int ph = 0; ph < 4; ++ph) {
      const int kk = ph >> 1, nh = ph & 1;
      if (nh == 0) {
#pragma unroll
        for (int m = 0; m < 4; ++m) af[m] = lds_b128(cur + rA[kk] + m * 2048);
      }
      s16x4 bl[5], bh[5];
#pragma unroll
      for (int n = 0; n < 5; ++n) {
        const int T = K1Tiles<WC>::T[nh * 5 + n];
        LDS_TR(bl[n], rBc[T & 3], ((kk * 4) * 5 + (T >> 2)) * 1024);
        LDS_TR(bh[n], rBc[T & 3], ((kk * 4) * 5 + (T >> 2)) * 1024 + 5120);
      }
      if (ph < 2) {
        if (moreA) { stageA(2 * ph, t + 1, oth); stageA(2 * ph + 1, t + 1, oth); }
      } else if (moreB) {
        if (ph == 2) { stageB(0, tb, bdst); stageB(1, tb, bdst); stageB(2, tb, bdst); }
        else { stageB(3, tb, bdst); stageB(4, tb, bdst); touch(t + 5 < K1_NT); }
      }
      if (WC == 1) {
        if (ph == 1) { if (moreA) WAIT_V(5); else WAIT_V(0); }
        if (ph == 3) { if (moreB) WAIT_V(6); else WAIT_V(0); }
      }
      asm volatile("s_waitcnt lgkmcnt(0)"
                   : "+v"(bl[0]), "+v"(bh[0]), "+v"(bl[1]), "+v"(bh[1]), "+v"(bl[2]), "+v"(bh[2]), "+v"(bl[3]), "+v"(bh[3]),
                     "+v"(bl[4]), "+v"(bh[4])::"memory");
      SCHED();
      BAR();
      SCHED();
#pragma unroll
      for (int n = 0; n < 5; ++n) bfv[n] = cat8(bl[n], bh[n]);
      __builtin_amdgcn_s_setprio(1);
#pragma unroll
      for (int n = 0; n < 5; ++n) {
        const int T = K1Tiles<WC>::T[nh * 5 + n];
#pragma unroll
        for (int m = 0; m < 4; ++m) {
          if (T >= 2 && T < 6) acc[m][nh * 5 + n] = MFMA(af[m], bfv[n], acc[m][nh * 5 + n]);
          else acc[m][nh * 5 + n] = MFMA(bfv[n], af[m], acc[m][nh * 5 + n]);
        }
      }
      __builtin_amdgcn_s_setprio(0);
      if (WC == 0 && ph == 3) { if (moreB) WAIT_V(6); else WAIT_V(0); }
      SCHED();
      BAR();
      SCHED();
    }
  } while (++t < nt);
  if (WC == 0) BAR();
  ST(0, 2);
  asm volatile("" ::"v"(dumT));
  const int row0 = 256 * r + 64 * wm;
  const int grow = 256 * r + 8 * c + w;
  u32x4 gx[2], gw[2];
#pragma unroll
  for (int q = 0; q < 2; ++q) {
    gx[q] = ((const u32x4*)(p.x + (size_t)grow * 2048))[lane * 2 + q];
    gw[q] = ((const u32x4*)p.gate_w)[lane * 2 + q];
  }
  if (WC == 1) {
#pragma unroll
    for (int m = 0; m < 4; ++m) {
      const f32x4 a = acc[m][0], b = acc[m][1];
      u32x4 v = {phi2(a[0], a[1]), phi2(a[2], a[3]), phi2(b[0], b[1]), phi2(b[2], b[3])};
      *(u32x4*)(p.pq + ((size_t)((row0 >> 4) + m) * 32 + c) * 1024 + lane * 16) = v;
    }
  } else {
    f32x4 kv[2][3];
#pragma unroll
    for (int dt = 0; dt < 2; ++dt)
#pragma unroll
      for (int et = 0; et < 3; ++et) kv[dt][et] = (f32x4){0.f, 0.f, 0.f, 0.f};
    const u32x4 ones = {0x3f803f80u, 0x3f803f80u, 0x3f803f80u, 0x3f803f80u};
#pragma unroll
    for (int ks = 0; ks < 2; ++ks) {
      bf16x8 kf[2], vf[3];
#pragma unroll
      for (int dt = 0; dt < 2; ++dt) {
        const f32x4 a = acc[2 * ks][dt], b = acc[2 * ks + 1][dt];
        u32x4 v = {phi2(a[0], a[1]), phi2(a[2], a[3]), phi2(b[0], b[1]), phi2(b[2], b[3])};
        kf[dt] = __builtin_bit_cast(bf16x8, v);
        const f32x4 a2 = acc[2 * ks][2 + dt], b2 = acc[2 * ks + 1][2 + dt];
        u32x4 v2 = {pack2(a2[0], a2[1]), pack2(a2[2], a2[3]), pack2(b2[0], b2[1]), pack2(b2[2], b2[3])};
        vf[dt] = __builtin_bit_cast(bf16x8, v2);
      }
      vf[2] = __builtin_bit_cast(bf16x8, ones);
#pragma unroll
      for (int dt = 0; dt < 2; ++dt)
#pragma unroll
        for (int et = 0; et < 3; ++et) kv[dt][et] = MFMA(kf[dt], vf[et], kv[dt][et]);
    }
#pragma unroll
    for (int dt = 0; dt < 2; ++dt)
#pragma unroll
      for (int et = 0; et < 3; ++et)
        *(__attribute__((address_space(3))) f32x4*)(uintptr_t)(sbase + ((wm * 6 + dt * 3 + et) * 64 + lane) * 16) = kv[dt][et];
  }
  WAIT_L(0);
  BAR();
  asm volatile("" ::: "memory");
  {
    const int tid = w * 64 + lane;
    if (tid < 384) {
      f32x4 s = (f32x4){0.f, 0.f, 0.f, 0.f};
#pragma unroll
      for (int q = 0; q < 4; ++q) s += *(__attribute__((address_space(3))) f32x4*)(uintptr_t)(sbase + (q * 384 + tid) * 16);
      *(f32x4*)(p.kvp + ((size_t)(r * 32 + c) * 384 + tid) * 4) = s;
    }
  }
  {
    constexpr int NP = WC == 0 ? 1 : 2;
#pragma unroll
    for (int pr = 0; pr < NP; ++pr) {
      const int s0 = WC == 0 ? 0 : 2 + 2 * pr;
      const int n0 = WC == 0 ? 4 : 2 + 4 * pr;
      const int j0 = (c & 1) ? (s0 < 2 ? s0 + 5 : s0 - 1) : s0;
      const int J = 7 * c + j0;
#pragma unroll
      for (int m = 0; m < 4; ++m) {
        const f32x4 g0 = acc[m][n0], u0 = acc[m][n0 + 1], g1 = acc[m][n0 + 2], u1 = acc[m][n0 + 3];
        u32x4 v = {mul2(g0[0], g0[1], u0[0], u0[1]), mul2(g0[2], g0[3], u0[2], u0[3]), mul2(g1[0], g1[1], u1[0], u1[1]),
                   mul2(g1[2], g1[3], u1[2], u1[3])};
        *(u32x4*)(p.a2 + ((size_t)(row0 + 16 * m + l15) * KA2 + 16 * J + 8 * g) * 2) = v;
      }
    }
    if (WC == 0) {
      const int J = 7 * c + ((c & 1) ? 0 : 6);
#pragma unroll
      for (int m = 0; m < 4; ++m) {
        const f32x4 g0 = acc[m][8], u0 = acc[m][9];
        u32x2 v = {mul2(g0[0], g0[1], u0[0], u0[1]), mul2(g0[2], g0[3], u0[2], u0[3])};
        *(u32x2*)(p.a2 + ((size_t)(row0 + 16 * m + l15) * KA2 + 32 * (J >> 1) + 8 * g + 4 * (J & 1)) * 2) = v;
      }
    }
  }
  {
    float sg = 0.f;
#pragma unroll
    for (int q = 0; q < 2; ++q)
#pragma unroll
      for (int e = 0; e < 4; ++e) {
        const unsigned pr = pack2(lo_f(gx[q][e]) * lo_f(gw[q][e]), hi_f(gx[q][e]) * hi_f(gw[q][e]));
        sg += lo_f(pr) + hi_f(pr);
      }
#pragma unroll
    for (int o = 32; o > 0; o >>= 1) sg += __shfl_xor(sg, o);
    if (lane == 0) {
      const float gg = bfr(1.0f / (1.0f + __expf(-bfr(sg))));
      p.gbuf[grow] = gg;
      p.gbuf[SEQ + grow] = bfr(1.0f - gg);
    }
  }
  ST(0, 3);
  ST(0, 4); ST(0, 7);
}

__global__ __launch_bounds__(512, 2) void k1_kernel(K1Args p) {
  __shared__ __attribute__((aligned(1024))) char smem[2 * K1_BUF];
  const int lane = threadIdx.x & 63;
  const int w = __builtin_amdgcn_readfirstlane(threadIdx.x >> 6);
  const int bid = blockIdx.x;
  const int xcd = bid & 7, idx = bid >> 3;
  const int r = idx & 7, c = xcd * 4 + (idx >> 3);
  const unsigned sbase = (unsigned)(uintptr_t)(__attribute__((address_space(3))) char*)smem;
  if (bid == 0) p.flags[threadIdx.x] = 0u;
  if (w < 4) k1_body<0>(p, sbase, lane, w, r, c);
  else k1_body<1>(p, sbase, lane, w, r, c);
}

#define K2_RS 1
#define K2_NO 40
constexpr int K2_SUB = 32768, K2_NH = 72 - K2_NO, K2_J0 = K2_NO - 16, K2_G0 = K2_J0 / 4;
static_assert(K2_NO % 4 == 0 && K2_G0 >= 4, "");

#define PIN_ACC()                                                                                                        \
  asm volatile("" : "+v"(acc[0][0]), "+v"(acc[0][1]), "+v"(acc[0][2]), "+v"(acc[0][3]), "+v"(acc[1][0]), "+v"(acc[1][1]), \
                    "+v"(acc[1][2]), "+v"(acc[1][3]), "+v"(acc[2][0]), "+v"(acc[2][1]), "+v"(acc[2][2]), "+v"(acc[2][3]), \
                    "+v"(acc[3][0]), "+v"(acc[3][1]), "+v"(acc[3][2]), "+v"(acc[3][3]))
#define WAIT_FRAG(a, l, h)                                                                                               \
  asm volatile("s_waitcnt lgkmcnt(0)" : "+v"(l[0]), "+v"(h[0]), "+v"(l[1]), "+v"(h[1]), "+v"(l[2]), "+v"(h[2]), "+v"(l[3]), \
               "+v"(h[3]), "+v"(a[0]), "+v"(a[1]), "+v"(a[2]), "+v"(a[3])::"memory")
#define LDS_RD(dst, addr, off) asm volatile("ds_read_b128 %0, %1 offset:%2" : "+v"(dst) : "v"(addr), "i"(off) : "memory")
#define LDS_TR2(dst, addr, off) asm volatile("ds_read_b64_tr_b16 %0, %1 offset:%2" : "+v"(dst) : "v"(addr), "i"(off) : "memory")
#define MFMA_ACC(c, x, y) asm volatile("v_mfma_f32_16x16x32_bf16 %0, %1, %2, %0" : "+v"(c) : "v"(x), "v"(y))
struct K2Args {
  char* a2; const char* wd; const char* wo; const float* gbuf; const char* pq; const float* kvp;
  float* xch; unsigned* flags; char* out;
};

__global__ __launch_bounds__(768, 3) void k2_kernel(K2Args p) {
  __shared__ __attribute__((aligned(1024))) char smem[4 * K2_SUB + 3072 + 4096];
  const int lane = threadIdx.x & 63;
  const int w = __builtin_amdgcn_readfirstlane(threadIdx.x >> 6);
  const int wm = w & 1, wn = (w >> 1) & 1;
  const int bid = blockIdx.x;
  const int xcd = bid & 7, idx = bid >> 3;
  const int s = xcd & 1, qd = xcd >> 1;
  const int tm = (qd >> 1) * 8 + (idx & 7), tn = (qd & 1) * 4 + (idx >> 3);
  const int tile = tm * 8 + tn;
  const int hi = qd * 32 + idx, ro = hi >> 5, hd = hi & 31;
  const unsigned sbase = (unsigned)(uintptr_t)(__attribute__((address_space(3))) char*)smem;
  const unsigned ksm = sbase + 4 * K2_SUB;
  const int g = lane >> 4, l15 = lane & 15, li = lane >> 3, pc = lane & 7;
  const int kb = s ? K2_J0 : 0, ns = s ? K2_NH : K2_NO;
  const int lw = w - 4;
  const char* abase = p.a2 + (size_t)(128 * tm + 16 * lw) * (KA2 * 2);
  const unsigned voffA0 = li * (KA2 * 2) + ((pc ^ (lane >> 4)) * 16), voffA1 = (voffA0 ^ 64) + 8 * KA2 * 2;
  const unsigned voffB = li * 2048 + (((pc >> 1) ^ (li >> 1)) * 32) + (pc & 1) * 16;
  const int boff = tn * 256 + lw * 16384;
  auto stage = [&](int j) {
    const unsigned buf = sbase + (j & 3) * 16384 + lw * 2048;
    if (!s && j >= K2_J0) j += K2_NH;
    j += kb;
    const char* bsrc = (j < 56 ? p.wd + (size_t)j * (64 * 2048) : p.wo + (size_t)(j - 56) * (64 * 2048)) + boff;
    glds_s(abase + j * 128, voffA0, buf);
    glds_s(abase + j * 128, voffA1, buf + 1024);
    glds_s(bsrc, voffB, buf + 65536);
    glds_s(bsrc + 128, voffB, buf + 65536 + 1024);
  };
  ST(1, 0); ST(1, 6);
  if (w >= 4) {
    if (s) {
      f32x4 kp[8];
      bf16x8 pf[4];
      float gv[4];
      const int tt = lw < 6 ? lw : 0;
      {
        const char* kq = (const char*)p.kvp + (size_t)(hd * 384 + tt * 64) * 16;
        const char* pq = p.pq + ((size_t)(32 * ro + 4 * lw) * 32 + hd) * 1024;
        const char* gq = (const char*)(p.gbuf + 512 * ro + 64 * lw);
        const unsigned v16 = lane * 16, v4 = l15 * 4;
#pragma unroll
        for (int q = 0; q < 8; ++q)
          asm volatile("global_load_dwordx4 %0, %1, %2" : "=v"(kp[q]) : "v"(v16), "s"(kq + q * (32 * 384 * 16)) : "memory");
#pragma unroll
        for (int m = 0; m < 4; ++m) {
          asm volatile("global_load_dwordx4 %0, %1, %2" : "=v"(pf[m]) : "v"(v16), "s"(pq + m * 32768) : "memory");
          asm volatile("global_load_dword %0, %1, %2 offset:%3" : "=v"(gv[m]) : "v"(v4), "s"(gq), "i"(m * 64) : "memory");
        }
      }
#pragma unroll
      for (int j = 0; j < 4; ++j) stage(j);
      asm volatile("s_waitcnt vmcnt(16)"
                   : "+v"(kp[0]), "+v"(kp[1]), "+v"(kp[2]), "+v"(kp[3]), "+v"(kp[4]), "+v"(kp[5]), "+v"(kp[6]), "+v"(kp[7]),
                     "+v"(pf[0]), "+v"(pf[1]), "+v"(pf[2]), "+v"(pf[3]), "+v"(gv[0]), "+v"(gv[1]), "+v"(gv[2]), "+v"(gv[3])::"memory");
      {
        const f32x4 sum = ((kp[0] + kp[1]) + (kp[2] + kp[3])) + ((kp[4] + kp[5]) + (kp[6] + kp[7]));
        const int dt = tt >= 3, et = tt - 3 * dt;
        u32x2 v = {pack2(sum[0], sum[1]), pack2(sum[2], sum[3])};
        *(__attribute__((address_space(3))) u32x2*)(uintptr_t)(ksm + (et * 64 + lane) * 16 + dt * 8) = v;
      }
      WAIT_L(0);
      BAR();
      asm volatile("" ::: "memory");
      bf16x8 kf[3];
#pragma unroll
      for (int et = 0; et < 3; ++et) kf[et] = lds_b128(ksm + (et * 64 + lane) * 16);
#pragma unroll
      for (int m = 0; m < 4; ++m) {
        const f32x4 z = {0.f, 0.f, 0.f, 0.f};
        f32x4 n0 = MFMA(kf[0], pf[m], z), n1 = MFMA(kf[1], pf[m], z), dn = MFMA(kf[2], pf[m], z);
        const float sc = gv[m] * __builtin_amdgcn_rcpf(dn[0] + 1e-6f);
        n0 *= sc;
        n1 *= sc;
        u32x4 v = {pack2(n0[0], n0[1]), pack2(n0[2], n0[3]), pack2(n1[0], n1[1]), pack2(n1[2], n1[3])};
        char* dst = p.a2 + ((size_t)(512 * ro + 64 * lw + 16 * m + l15) * KA2 + FF + 32 * hd + 8 * g) * 2;
        asm volatile("global_store_dwordx4 %0, %1, off sc1\n\ts_nop 1" ::"v"(dst), "v"(v) : "memory");
      }
      WAIT_V(16);
      BAR();
      WAIT_V(12);
    } else {
#pragma unroll
      for (int j = 0; j < 4; ++j) stage(j);
      WAIT_V(12);
      BAR();
      WAIT_V(8);
    }
    BAR();
    for (int j = 0; j < ns - 1; ++j) {
      if (j + 4 < ns) { stage(j + 4); if (s && j < 2) WAIT_V(12); else WAIT_V(8); }
      else if (j + 4 == ns) WAIT_V(4);
      else WAIT_V(0);
      BAR();
    }
    return;
  }
  {
    f32x4 omg;
#pragma unroll
    for (int m = 0; m < 4; ++m) omg[m] = p.gbuf[SEQ + 128 * tm + 64 * wm + 16 * m + l15];
    *(__attribute__((address_space(3))) f32x4*)(uintptr_t)(ksm + 3072 + threadIdx.x * 16) = omg;
  }
  f32x4 acc[4][4];
#pragma unroll
  for (int m = 0; m < 4; ++m)
#pragma unroll
    for (int n = 0; n < 4; ++n) acc[m][n] = (f32x4){0.f, 0.f, 0.f, 0.f};
  unsigned rA[2], rB[4];
  rA[0] = sbase + (64 * wm + l15) * 128 + ((g ^ (l15 >> 1)) * 16);
  rA[1] = rA[0] ^ 64;
  {
    const int i = (g & 1) * 4 + (l15 >> 2), pp = lane & 3;
#pragma unroll
    for (int tp = 0; tp < 4; ++tp)
      rB[tp] = sbase + 65536 + ((g >> 1) * 2 + wn) * 1024 + i * 128 + ((tp ^ (i >> 1)) * 2 + (pp >> 1)) * 16 + (pp & 1) * 8;
  }
  PIN_ACC();
  asm volatile("s_waitcnt vmcnt(0) lgkmcnt(0)" : "+v"(rA[0]), "+v"(rA[1]), "+v"(rB[0]), "+v"(rB[1]), "+v"(rB[2]), "+v"(rB[3])::"memory");
  const int ng = s ? K2_NH / 4 : K2_G0;
  const unsigned endm = (1u << (ng - 1)) | (w == 0 ? (s ? 1u : (7u << (K2_G0 - 4))) : 0u);
  if (s) BAR();
  BAR();
  ST(1, 1);
  bf16x8 a0[4] = {}, a1[4] = {};
  s16x4 l0[4] = {}, h0[4] = {}, l1[4] = {}, h1[4] = {};
#pragma unroll
  for (int q = 0; q < 4; ++q) {
    LDS_RD(a0[q], rA[0], q * 2048);
    LDS_TR(l0[q], rB[q], 0);
    LDS_TR(h0[q], rB[q], 4096);
  }
  WAIT_FRAG(a0, l0, h0);
#define K2_READ(r, a, l, h, u, kh)                                                                          \
  do {                                                                                                      \
    if ((r) % 3 == 0) LDS_RD(a[(r) / 3], rA[kh], (u) * 16384 + ((r) / 3) * 2048); \
    else if ((r) % 3 == 1) LDS_TR2(l[(r) / 3], rB[(r) / 3], (u) * 16384 + (kh) * 8192); \
    else LDS_TR2(h[(r) / 3], rB[(r) / 3], (u) * 16384 + (kh) * 8192 + 4096); \
  } while (0)
  auto group = [&]() __attribute__((always_inline)) {
#pragma unroll
    for (int u = 0; u < 4; ++u) {
      SCHED();
#pragma unroll
      for (int t = 0; t < 16; ++t) {
        MFMA_ACC(acc[t & 3][t >> 2], cat8(l0[t >> 2], h0[t >> 2]), a0[t & 3]);
        SCHED();
#pragma unroll
        for (int r = t * K2_RS; r < (t + 1) * K2_RS; ++r)
          if (r < 12) K2_READ(r, a1, l1, h1, u, 1);
        SCHED();
      }
      WAIT_FRAG(a1, l1, h1);
      BAR();
      SCHED();
#pragma unroll
      for (int t = 0; t < 16; ++t) {
        MFMA_ACC(acc[t & 3][t >> 2], cat8(l1[t >> 2], h1[t >> 2]), a1[t & 3]);
        SCHED();
#pragma unroll
        for (int r = t * K2_RS; r < (t + 1) * K2_RS; ++r)
          if (r < 12) K2_READ(r, a0, l0, h0, (u + 1) & 3, 0);
        SCHED();
      }
      WAIT_FRAG(a0, l0, h0);
    }
    SCHED();
  };
  for (int jg = 0;;) {
    group();
    if (!((endm >> jg++) & 1u)) continue;
    if (jg == ng) break;
    if (s) {
      if (threadIdx.x == 0) __hip_atomic_fetch_add((gu32*)(p.flags + ro * 32), 1u, __ATOMIC_RELAXED, __HIP_MEMORY_SCOPE_AGENT);
    } else {
      gu32* f = (gu32*)(p.flags + (tm >> 2) * 32);
      if (jg == K2_G0 - 3) {
        asm volatile("s_mov_b32 m0, %1\n\ts_nop 0\n\tglobal_load_lds_dword %0, off sc1" ::"v"(f), "s"(ksm) : "memory");
      } else if (jg == K2_G0 - 2) {
        WAIT_V(0);
        if (*(volatile __attribute__((address_space(3))) unsigned*)(uintptr_t)ksm < 32u)
          while (__hip_atomic_load(f, __ATOMIC_RELAXED, __HIP_MEMORY_SCOPE_AGENT) < 32u) __builtin_amdgcn_s_sleep(2);
        asm volatile("buffer_inv sc1" ::: "memory");
      } else {
        WAIT_V(0);
      }
    }
  }
  int tid = threadIdx.x;
  asm volatile("s_nop 7\n\ts_nop 7" : "+v"(tid));
  PIN_ACC();
  {
    const f32x4 omg = *(__attribute__((address_space(3))) f32x4*)(uintptr_t)(ksm + 3072 + tid * 16);
#pragma unroll
    for (int m = 0; m < 4; ++m)
#pragma unroll
      for (int n = 0; n < 4; ++n) acc[m][n] *= omg[m];
  }
  if (!s) {
    PIN_ACC();
    asm volatile("s_nop 3");
#pragma nounroll
    for (int jg = 0; jg < 4; ++jg) group();
    asm volatile("s_nop 7\n\ts_nop 7" : "+v"(tid));
    PIN_ACC();
  }
  ST(1, 2);
  {
    const int lane = tid & 63, g = lane >> 4, l15 = lane & 15;
    gu32* f = (gu32*)(p.flags + 256 + tile);
    if (s) {
      float* xw = p.xch + ((size_t)tile * 4 + w) * 4096 + lane * 4;
#pragma unroll
      for (int q = 0; q < 16; ++q)
        asm volatile("global_store_dwordx4 %0, %1, off sc1\n\ts_nop 1" ::"v"(xw + q * 256), "v"(acc[q >> 2][q & 3]) : "memory");
      ST(1, 3);
      WAIT_V(0);
      BAR();
      if (tid == 0) __hip_atomic_store(f, 1u, __ATOMIC_RELAXED, __HIP_MEMORY_SCOPE_AGENT);
    } else {
      while (__hip_atomic_load(f, __ATOMIC_RELAXED, __HIP_MEMORY_SCOPE_AGENT) == 0u) __builtin_amdgcn_s_sleep(1);
      const __amdgpu_buffer_rsrc_t xr =
          __builtin_amdgcn_make_buffer_rsrc((void*)(p.xch + ((size_t)tile * 4 + w) * 4096), 0, 16384, 0x00020000);
      f32x4 o[16];
#pragma unroll
      for (int q = 0; q < 16; ++q)
        o[q] = __builtin_bit_cast(f32x4, __builtin_amdgcn_raw_buffer_load_b128(xr, lane * 16 + q * 1024, 0, 16));
      ST(1, 3);
#pragma unroll
      for (int q = 0; q < 8; ++q) {
        const int m = q >> 1, n = (q & 1) * 2;
        const f32x4 x = acc[m][n] + o[2 * q], y = acc[m][n + 1] + o[2 * q + 1];
        const auto r0 = __builtin_amdgcn_permlane16_swap(pack2(x[0], x[1]), pack2(y[0], y[1]), false, false);
        const auto r1 = __builtin_amdgcn_permlane16_swap(pack2(x[2], x[3]), pack2(y[2], y[3]), false, false);
        const u32x4 pk = {r0[0], r1[0], r0[1], r1[1]};
        const int row = 128 * tm + 64 * wm + 16 * m + l15;
        *(u32x4*)(p.out + ((size_t)row * DM + 128 * tn + 64 * wn + 16 * n + 16 * (g & 1) + 8 * (g >> 1)) * 2) = pk;
      }
    }
  }
  ST(1, 4); ST(1, 7);
}

extern "C" void kernel_launch(void* const* d_in, const int* in_sizes, int n_in, void* d_out, int out_size, void* d_ws,
                              size_t ws_size, hipStream_t stream) {
  char* ws = (char*)d_ws;
  unsigned* flags = (unsigned*)ws;
  float* gbuf = (float*)(ws + 4096);
  float* kvp = (float*)(ws + 65536);
  char* pq = ws + (2u << 20);
  char* a2 = ws + (8u << 20);
  float* xch = (float*)(ws + (32u << 20));
  K1Args a;
  a.x = (const char*)d_in[0]; a.wqkv = (const char*)d_in[1]; a.wg = (const char*)d_in[4]; a.wu = (const char*)d_in[5];
  a.gate_w = (const char*)d_in[7]; a.a2 = a2; a.pq = pq; a.kvp = kvp; a.gbuf = gbuf; a.flags = flags;
  a.wd = (const char*)d_in[6]; a.wo = (const char*)d_in[2];
  k1_kernel<<<256, 512, 0, stream>>>(a);
  K2Args b;
  b.a2 = a2; b.wd = (const char*)d_in[6]; b.wo = (const char*)d_in[2]; b.gbuf = gbuf; b.pq = pq; b.kvp = kvp; b.xch = xch; b.flags = flags;
  b.out = (char*)d_out;
  k2_kernel<<<256, 768, 0, stream>>>(b);
}
```

```cpp
#include <hip/hip_runtime.h>
#include <hip/hip_bf16.h>
#include <stdint.h>

typedef __attribute__((ext_vector_type(8))) short bf16x8;
typedef __attribute__((ext_vector_type(4))) short s16x4;
typedef __attribute__((ext_vector_type(4))) float f32x4;
typedef __attribute__((ext_vector_type(4))) unsigned u32x4;
typedef __attribute__((ext_vector_type(2))) unsigned u32x2;
typedef __attribute__((ext_vector_type(2))) float f32x2;
typedef __attribute__((ext_vector_type(2))) __bf16 bf16x2_t;
typedef __attribute__((address_space(3))) s16x4* lds_s16x4_p;
typedef __attribute__((address_space(3))) void* lds_void_p;
typedef __attribute__((address_space(1))) const void* glb_cvoid_p;
typedef __attribute__((address_space(1))) unsigned gu32;

#define DEV __device__ __forceinline__
#define BAR() __builtin_amdgcn_s_barrier()
#define SCHED() __builtin_amdgcn_sched_barrier(0)
#define WAIT_V(n) asm volatile("s_waitcnt vmcnt(" #n ")" ::: "memory")
#define WAIT_L(n) asm volatile("s_waitcnt lgkmcnt(" #n ")" ::: "memory")
#define MFMA(a, b, c) __builtin_amdgcn_mfma_f32_16x16x32_bf16(a, b, c, 0, 0, 0)

constexpr int SEQ = 2048, DM = 1024, FF = 3584, KA2 = 4608;

DEV unsigned pack2(float a, float b) {
  f32x2 v = {a, b};
  bf16x2_t r = __builtin_convertvector(v, bf16x2_t);
  return __builtin_bit_cast(unsigned, r);
}
DEV float bfr(float a) { return (float)(__bf16)a; }
DEV float lo_f(unsigned u) { return __builtin_bit_cast(float, u << 16); }
DEV float hi_f(unsigned u) { return __builtin_bit_cast(float, u & 0xffff0000u); }
DEV unsigned phi2(float a, float b) {
  const float ya = a > 0.f ? a + 1.0f : __expf(a), yb = b > 0.f ? b + 1.0f : __expf(b);
  return pack2(ya + 1e-6f, yb + 1e-6f);
}
DEV unsigned mul2(float g0, float g1, float u0, float u1) { return pack2(g0 * u0, g1 * u1); }
DEV void glds_s(const char* ubase, unsigned voff, unsigned lds_addr) {
  asm volatile("s_mov_b32 m0, %2\n\ts_nop 0\n\tglobal_load_lds_dwordx4 %0, %1" ::"v"(voff), "s"(ubase), "s"(lds_addr)
               : "memory");
}
DEV void glds_v(const char* vptr, unsigned lds_addr) {
  asm volatile("s_mov_b32 m0, %1\n\ts_nop 0\n\tglobal_load_lds_dwordx4 %0, off" ::"v"(vptr), "s"(lds_addr)
               : "memory");
}
DEV bf16x8 lds_b128(unsigned addr) { return *(__attribute__((address_space(3))) bf16x8*)(uintptr_t)addr; }
#define LDS_TR(dst, addr, off) asm volatile("ds_read_b64_tr_b16 %0, %1 offset:%2" : "=v"(dst) : "v"(addr), "i"(off) : "memory")
DEV bf16x8 cat8(s16x4 a, s16x4 b) { return __builtin_shufflevector(a, b, 0, 1, 2, 3, 4, 5, 6, 7); }

constexpr int K1_A = 32768, K1_BUF = 73728, K1_NT = DM / 64;

template <int WC> struct K1Tiles;
template <> struct K1Tiles<0> { static constexpr int T[10] = {2, 3, 4, 5, 6, 13, 7, 14, 12, 19}; };
template <> struct K1Tiles<1> { static constexpr int T[10] = {0, 1, 8, 15, 9, 16, 10, 17, 11, 18}; };

struct K1Args {
  const char* x; const char* wqkv; const char* wg; const char* wu; const char* gate_w;
  char* a2; char* pq; float* kvp; float* gbuf; unsigned* flags; const char* wd; const char* wo;
};

template <int WC>
DEV void k1_body(const K1Args& p, unsigned sbase, int lane, int w, int r, int c) {
  const int wm = w & 3;
  const int g = lane >> 4, l15 = lane & 15;
  const int li = lane >> 3, pc = lane & 7;
  const char* xbase = p.x + (size_t)(256 * r + 32 * w) * 2048;
  unsigned voffA0 = li * 2048 + ((pc ^ (lane >> 4)) * 16);
  unsigned voffA1 = voffA0 ^ 64;
  const char* pB1; const char* pB3;
  unsigned incB1, voffB[3];
  {
    const int krow = 32 * (w >> 2) + 16 * ((w >> 1) & 1) + 4 * (w & 1) + (li & 3) + 8 * (li >> 2);
    const int tp = (pc >> 1) ^ (li >> 1);
    auto off_qkv = [&](int T) { return (unsigned)(krow * 6144 + ((T >> 1) * 1024 + 32 * c + 16 * (T & 1) + 8 * (pc & 1)) * 2); };
    auto off_gu = [&](int sj) {
      const int j = (c & 1) ? (sj < 2 ? sj + 5 : (sj < 6 ? sj - 1 : 0)) : sj;
      return (unsigned)(krow * 7168 + (112 * c + 16 * j + 8 * (pc & 1)) * 2);
    };
    voffB[0] = off_qkv(tp);
    voffB[1] = off_gu(2 + tp);
    voffB[2] = off_gu(3 + tp);
    pB1 = tp < 2 ? p.wqkv + off_qkv(4 + tp) : p.wg + off_gu(tp - 2);
    incB1 = tp < 2 ? 64 * 6144 : 64 * 7168;
    pB3 = tp < 1 ? p.wg + off_gu(6) : p.wu + off_gu(tp - 1);
  }
  unsigned rA[2], rB[4];
  rA[0] = (64 * wm + l15) * 128 + ((g ^ (l15 >> 1)) * 16);
  rA[1] = rA[0] ^ 64;
  {
    const int i = (g & 1) * 4 + (l15 >> 2), pp = lane & 3;
#pragma unroll
    for (int tp = 0; tp < 4; ++tp)
      rB[tp] = K1_A + (g >> 1) * 10240 + i * 128 + ((tp ^ (i >> 1)) * 2 + (pp >> 1)) * 16 + (pp & 1) * 8;
  }
  auto stageA = [&](int i, int kt, unsigned buf) {
    glds_s(xbase + i * 16384 + kt * 128, (i & 1) ? voffA1 : voffA0, buf + (4 * w + i) * 1024);
  };
  auto stageB = [&](int G, int kt, unsigned buf) {
    const unsigned dst = buf + K1_A + (w * 5 + G) * 1024;
    if (G == 0) glds_s(p.wqkv + (size_t)kt * (64 * 6144), voffB[0], dst);
    if (G == 2) glds_s(p.wg + (size_t)kt * (64 * 7168), voffB[1], dst);
    if (G == 4) glds_s(p.wu + (size_t)kt * (64 * 7168), voffB[2], dst);
    if (G == 1) { glds_v(pB1, dst); pB1 += incB1; }
    if (G == 3) { glds_v(pB3, dst); pB3 += 64 * 7168; }
  };

  f32x4 acc[4][10];
#pragma unroll
  for (int m = 0; m < 4; ++m)
#pragma unroll
    for (int n = 0; n < 10; ++n) acc[m][n] = (f32x4){0.f, 0.f, 0.f, 0.f};

#pragma unroll
  for (int i = 0; i < 4; ++i) stageA(i, 0, sbase);
#pragma unroll
  for (int G = 0; G < 5; ++G) stageB(G, 0, sbase);
  if (WC == 0) {
#pragma unroll
    for (int G = 0; G < 5; ++G) stageB(G, 1, sbase + K1_BUF);
  }
  SCHED();
  const char* pT;
  unsigned incT, dumT = 0;
  {
    const int wid = blockIdx.x * 8 + w;
    const int H = wid * 6 + (lane < 6 ? lane : 0);
    const int H1 = H - 2048, H2 = H1 - 3072, H3 = H2 - 3584;
    pT = H1 < 0 ? p.x + H * 2048 : (H2 < 0 ? p.wqkv + H1 * 128 : (H3 < 0 ? p.wg + H2 * 128 : p.wu + H3 * 128));
    incT = H1 < 0 ? 128 : (H2 < 0 ? 64 * 6144 : 64 * 7168);
    pT += 3 * incT;
    if (lane >= 6 && lane < 9) {
      const int sid = wid * 3 + lane - 6, s1 = sid * 13, s2 = (sid - 4412) * 13;
      pT = sid < 4412 ? p.wd + (s1 < 57344 - 13 ? s1 : 57344 - 13) * 128 : p.wo + (s2 < 16384 - 13 ? s2 : 16384 - 13) * 128;
      incT = 128;
    }
  }
  auto touch = [&](bool adv) {
    asm volatile("s_mov_b64 exec, 0x1ff\n\tglobal_load_dword %0, %1, off\n\ts_mov_b64 exec, -1" : "+v"(dumT) : "v"(pT) : "memory");
    pT += adv ? incT : 0u;
  };
  touch(true);
#pragma unroll
  for (int m = 0; m < 4; ++m)
    asm volatile("" : "+v"(acc[m][0]), "+v"(acc[m][1]), "+v"(acc[m][2]), "+v"(acc[m][3]), "+v"(acc[m][4]), "+v"(acc[m][5]),
                      "+v"(acc[m][6]), "+v"(acc[m][7]), "+v"(acc[m][8]), "+v"(acc[m][9]));
  WAIT_V(6);
  BAR();
  if (WC == 1) BAR();

  bf16x8 af[4], bfv[5];
  int nt = K1_NT;
  asm volatile("" : "+s"(nt));
  int t = 0;
  do {
    const unsigned cur = sbase + (t & 1) * K1_BUF, oth = sbase + ((t & 1) ^ 1) * K1_BUF;
    const bool moreA = t + 1 < nt;
    const bool moreB = WC == 0 ? (t + 2 < nt) : (t + 1 < nt);
    const unsigned bdst = WC == 0 ? cur : oth;
    const int tb = WC == 0 ? t + 2 : t + 1;
    unsigned rBc[4];
#pragma unroll
    for (int q = 0; q < 4; ++q) rBc[q] = cur + rB[q];
#pragma unroll
    for (int ph = 0; ph < 4; ++ph) {
      const int kk = ph >> 1, nh = ph & 1;
      if (nh == 0) {
#pragma unroll
        for (int m = 0; m < 4; ++m) af[m] = lds_b128(cur + rA[kk] + m * 2048);
      }
      s16x4 bl[5], bh[5];
#pragma unroll
      for (int n = 0; n < 5; ++n) {
        const int T = K1Tiles<WC>::T[nh * 5 + n];
        LDS_TR(bl[n], rBc[T & 3], ((kk * 4) * 5 + (T >> 2)) * 1024);
        LDS_TR(bh[n], rBc[T & 3], ((kk * 4) * 5 + (T >> 2)) * 1024 + 5120);
      }
      if (ph < 2) {
        if (moreA) { stageA(2 * ph, t + 1, oth); stageA(2 * ph + 1, t + 1, oth); }
      } else if (moreB) {
        if (ph == 2) { stageB(0, tb, bdst); stageB(1, tb, bdst); stageB(2, tb, bdst); }
        else { stageB(3, tb, bdst); stageB(4, tb, bdst); touch(t + 5 < K1_NT); }
      }
      if (WC == 1) {
        if (ph == 1) { if (moreA) WAIT_V(5); else WAIT_V(0); }
        if (ph == 3) { if (moreB) WAIT_V(6); else WAIT_V(0); }
      }
      asm volatile("s_waitcnt lgkmcnt(0)"
                   : "+v"(bl[0]), "+v"(bh[0]), "+v"(bl[1]), "+v"(bh[1]), "+v"(bl[2]), "+v"(bh[2]), "+v"(bl[3]), "+v"(bh[3]),
                     "+v"(bl[4]), "+v"(bh[4])::"memory");
      SCHED();
      BAR();
      SCHED();
#pragma unroll
      for (int n = 0; n < 5; ++n) bfv[n] = cat8(bl[n], bh[n]);
      __builtin_amdgcn_s_setprio(1);
#pragma unroll
      for (int n = 0; n < 5; ++n) {
        const int T = K1Tiles<WC>::T[nh * 5 + n];
#pragma unroll
        for (int m = 0; m < 4; ++m) {
          if (T >= 2 && T < 6) acc[m][nh * 5 + n] = MFMA(af[m], bfv[n], acc[m][nh * 5 + n]);
          else acc[m][nh * 5 + n] = MFMA(bfv[n], af[m], acc[m][nh * 5 + n]);
        }
      }
      __builtin_amdgcn_s_setprio(0);
      if (WC == 0 && ph == 3) { if (moreB) WAIT_V(6); else WAIT_V(0); }
      SCHED();
      BAR();
      SCHED();
    }
  } while (++t < nt);
  if (WC == 0) BAR();
  asm volatile("" ::"v"(dumT));
  const int row0 = 256 * r + 64 * wm;
  const int grow = 256 * r + 8 * c + w;
  u32x4 gx[2], gw[2];
#pragma unroll
  for (int q = 0; q < 2; ++q) {
    gx[q] = ((const u32x4*)(p.x + (size_t)grow * 2048))[lane * 2 + q];
    gw[q] = ((const u32x4*)p.gate_w)[lane * 2 + q];
  }
  if (WC == 1) {
#pragma unroll
    for (int m = 0; m < 4; ++m) {
      const f32x4 a = acc[m][0], b = acc[m][1];
      u32x4 v = {phi2(a[0], a[1]), phi2(a[2], a[3]), phi2(b[0], b[1]), phi2(b[2], b[3])};
      *(u32x4*)(p.pq + ((size_t)((row0 >> 4) + m) * 32 + c) * 1024 + lane * 16) = v;
    }
  } else {
    f32x4 kv[2][3];
#pragma unroll
    for (int dt = 0; dt < 2; ++dt)
#pragma unroll
      for (int et = 0; et < 3; ++et) kv[dt][et] = (f32x4){0.f, 0.f, 0.f, 0.f};
    const u32x4 ones = {0x3f803f80u, 0x3f803f80u, 0x3f803f80u, 0x3f803f80u};
#pragma unroll
    for (int ks = 0; ks < 2; ++ks) {
      bf16x8 kf[2], vf[3];
#pragma unroll
      for (int dt = 0; dt < 2; ++dt) {
        const f32x4 a = acc[2 * ks][dt], b = acc[2 * ks + 1][dt];
        u32x4 v = {phi2(a[0], a[1]), phi2(a[2], a[3]), phi2(b[0], b[1]), phi2(b[2], b[3])};
        kf[dt] = __builtin_bit_cast(bf16x8, v);
        const f32x4 a2 = acc[2 * ks][2 + dt], b2 = acc[2 * ks + 1][2 + dt];
        u32x4 v2 = {pack2(a2[0], a2[1]), pack2(a2[2], a2[3]), pack2(b2[0], b2[1]), pack2(b2[2], b2[3])};
        vf[dt] = __builtin_bit_cast(bf16x8, v2);
      }
      vf[2] = __builtin_bit_cast(bf16x8, ones);
#pragma unroll
      for (int dt = 0; dt < 2; ++dt)
#pragma unroll
        for (int et = 0; et < 3; ++et) kv[dt][et] = MFMA(kf[dt], vf[et], kv[dt][et]);
    }
#pragma unroll
    for (int dt = 0; dt < 2; ++dt)
#pragma unroll
      for (int et = 0; et < 3; ++et)
        *(__attribute__((address_space(3))) f32x4*)(uintptr_t)(sbase + ((wm * 6 + dt * 3 + et) * 64 + lane) * 16) = kv[dt][et];
  }
  WAIT_L(0);
  BAR();
  asm volatile("" ::: "memory");
  {
    const int tid = w * 64 + lane;
    if (tid < 384) {
      f32x4 s = (f32x4){0.f, 0.f, 0.f, 0.f};
#pragma unroll
      for (int q = 0; q < 4; ++q) s += *(__attribute__((address_space(3))) f32x4*)(uintptr_t)(sbase + (q * 384 + tid) * 16);
      *(f32x4*)(p.kvp + ((size_t)(r * 32 + c) * 384 + tid) * 4) = s;
    }
  }
  {
    constexpr int NP = WC == 0 ? 1 : 2;
#pragma unroll
    for (int pr = 0; pr < NP; ++pr) {
      const int s0 = WC == 0 ? 0 : 2 + 2 * pr;
      const int n0 = WC == 0 ? 4 : 2 + 4 * pr;
      const int j0 = (c & 1) ? (s0 < 2 ? s0 + 5 : s0 - 1) : s0;
      const int J = 7 * c + j0;
#pragma unroll
      for (int m = 0; m < 4; ++m) {
        const f32x4 g0 = acc[m][n0], u0 = acc[m][n0 + 1], g1 = acc[m][n0 + 2], u1 = acc[m][n0 + 3];
        u32x4 v = {mul2(g0[0], g0[1], u0[0], u0[1]), mul2(g0[2], g0[3], u0[2], u0[3]), mul2(g1[0], g1[1], u1[0], u1[1]),
                   mul2(g1[2], g1[3], u1[2], u1[3])};
        *(u32x4*)(p.a2 + ((size_t)(row0 + 16 * m + l15) * KA2 + 16 * J + 8 * g) * 2) = v;
      }
    }
    if (WC == 0) {
      const int J = 7 * c + ((c & 1) ? 0 : 6);
#pragma unroll
      for (int m = 0; m < 4; ++m) {
        const f32x4 g0 = acc[m][8], u0 = acc[m][9];
        u32x2 v = {mul2(g0[0], g0[1], u0[0], u0[1]), mul2(g0[2], g0[3], u0[2], u0[3])};
        *(u32x2*)(p.a2 + ((size_t)(row0 + 16 * m + l15) * KA2 + 32 * (J >> 1) + 8 * g + 4 * (J & 1)) * 2) = v;
      }
    }
  }
  {
    float sg = 0.f;
#pragma unroll
    for (int q = 0; q < 2; ++q)
#pragma unroll
      for (int e = 0; e < 4; ++e) {
        const unsigned pr = pack2(lo_f(gx[q][e]) * lo_f(gw[q][e]), hi_f(gx[q][e]) * hi_f(gw[q][e]));
        sg += lo_f(pr) + hi_f(pr);
      }
#pragma unroll
    for (int o = 32; o > 0; o >>= 1) sg += __shfl_xor(sg, o);
    if (lane == 0) {
      const float gg = bfr(1.0f / (1.0f + __expf(-bfr(sg))));
      p.gbuf[grow] = gg;
      p.gbuf[SEQ + grow] = bfr(1.0f - gg);
    }
  }
}

__global__ __launch_bounds__(512, 2) void k1_kernel(K1Args p) {
  __shared__ __attribute__((aligned(1024))) char smem[2 * K1_BUF];
  const int lane = threadIdx.x & 63;
  const int w = __builtin_amdgcn_readfirstlane(threadIdx.x >> 6);
  const int bid = blockIdx.x;
  const int xcd = bid & 7, idx = bid >> 3;
  const int r = idx & 7, c = xcd * 4 + (idx >> 3);
  const unsigned sbase = (unsigned)(uintptr_t)(__attribute__((address_space(3))) char*)smem;
  if (bid == 0) p.flags[threadIdx.x] = 0u;
  if (w < 4) k1_body<0>(p, sbase, lane, w, r, c);
  else k1_body<1>(p, sbase, lane, w, r, c);
}

constexpr int K2_RS = 1, K2_NO = 40;
constexpr int K2_SUB = 32768, K2_NH = 72 - K2_NO, K2_J0 = K2_NO - 16, K2_G0 = K2_J0 / 4;

#define PIN_ACC()                                                                                                        \
  asm volatile("" : "+v"(acc[0][0]), "+v"(acc[0][1]), "+v"(acc[0][2]), "+v"(acc[0][3]), "+v"(acc[1][0]), "+v"(acc[1][1]), \
                    "+v"(acc[1][2]), "+v"(acc[1][3]), "+v"(acc[2][0]), "+v"(acc[2][1]), "+v"(acc[2][2]), "+v"(acc[2][3]), \
                    "+v"(acc[3][0]), "+v"(acc[3][1]), "+v"(acc[3][2]), "+v"(acc[3][3]))
#define WAIT_FRAG(a, l, h)                                                                                               \
  asm volatile("s_waitcnt lgkmcnt(0)" : "+v"(l[0]), "+v"(h[0]), "+v"(l[1]), "+v"(h[1]), "+v"(l[2]), "+v"(h[2]), "+v"(l[3]), \
               "+v"(h[3]), "+v"(a[0]), "+v"(a[1]), "+v"(a[2]), "+v"(a[3])::"memory")
#define LDS_RD(dst, addr, off) asm volatile("ds_read_b128 %0, %1 offset:%2" : "+v"(dst) : "v"(addr), "i"(off) : "memory")
#define LDS_TR2(dst, addr, off) asm volatile("ds_read_b64_tr_b16 %0, %1 offset:%2" : "+v"(dst) : "v"(addr), "i"(off) : "memory")
#define MFMA_ACC(c, x, y) asm volatile("v_mfma_f32_16x16x32_bf16 %0, %1, %2, %0" : "+v"(c) : "v"(x), "v"(y))
struct K2Args {
  char* a2; const char* wd; const char* wo; const float* gbuf; const char* pq; const float* kvp;
  float* xch; unsigned* flags; char* out;
};

__global__ __launch_bounds__(768, 3) void k2_kernel(K2Args p) {
  __shared__ __attribute__((aligned(1024))) char smem[4 * K2_SUB + 3072 + 4096];
  const int lane = threadIdx.x & 63;
  const int w = __builtin_amdgcn_readfirstlane(threadIdx.x >> 6);
  const int wm = w & 1, wn = (w >> 1) & 1;
  const int bid = blockIdx.x;
  const int xcd = bid & 7, idx = bid >> 3;
  const int s = xcd & 1, qd = xcd >> 1;
  const int tm = (qd >> 1) * 8 + (idx & 7), tn = (qd & 1) * 4 + (idx >> 3);
  const int tile = tm * 8 + tn;
  const int hi = qd * 32 + idx, ro = hi >> 5, hd = hi & 31;
  const unsigned sbase = (unsigned)(uintptr_t)(__attribute__((address_space(3))) char*)smem;
  const unsigned ksm = sbase + 4 * K2_SUB;
  const int g = lane >> 4, l15 = lane & 15, li = lane >> 3, pc = lane & 7;
  const int kb = s ? K2_J0 : 0, ns = s ? K2_NH : K2_NO;
  const int lw = w - 4;
  const char* abase = p.a2 + (size_t)(128 * tm + 16 * lw) * (KA2 * 2);
  const unsigned voffA0 = li * (KA2 * 2) + ((pc ^ (lane >> 4)) * 16), voffA1 = (voffA0 ^ 64) + 8 * KA2 * 2;
  const unsigned voffB = li * 2048 + (((pc >> 1) ^ (li >> 1)) * 32) + (pc & 1) * 16;
  const int boff = tn * 256 + lw * 16384;
  auto stage = [&](int j) {
    const unsigned buf = sbase + (j & 3) * 16384 + lw * 2048;
    if (!s && j >= K2_J0) j += K2_NH;
    j += kb;
    const char* bsrc = (j < 56 ? p.wd + (size_t)j * (64 * 2048) : p.wo + (size_t)(j - 56) * (64 * 2048)) + boff;
    glds_s(abase + j * 128, voffA0, buf);
    glds_s(abase + j * 128, voffA1, buf + 1024);
    glds_s(bsrc, voffB, buf + 65536);
    glds_s(bsrc + 128, voffB, buf + 65536 + 1024);
  };
  if (w >= 4) {
#pragma unroll
    for (int j = 0; j < 4; ++j) stage(j);
    if (s) {
      f32x4 kp[8];
      bf16x8 pf[4];
      float gv[4];
      const int tt = lw < 6 ? lw : 0;
#pragma unroll
      for (int q = 0; q < 8; ++q) kp[q] = *(const f32x4*)(p.kvp + ((size_t)(q * 32 + hd) * 384 + tt * 64 + lane) * 4);
#pragma unroll
      for (int m = 0; m < 4; ++m) {
        pf[m] = *(const bf16x8*)(p.pq + ((size_t)(32 * ro + 4 * lw + m) * 32 + hd) * 1024 + lane * 16);
        gv[m] = p.gbuf[512 * ro + 64 * lw + 16 * m + l15];
      }
      {
        const f32x4 sum = ((kp[0] + kp[1]) + (kp[2] + kp[3])) + ((kp[4] + kp[5]) + (kp[6] + kp[7]));
        const int dt = tt >= 3, et = tt - 3 * dt;
        u32x2 v = {pack2(sum[0], sum[1]), pack2(sum[2], sum[3])};
        if (lw < 6) *(__attribute__((address_space(3))) u32x2*)(uintptr_t)(ksm + (et * 64 + lane) * 16 + dt * 8) = v;
      }
      asm volatile("s_waitcnt vmcnt(0) lgkmcnt(0)" : "+v"(pf[0]), "+v"(pf[1]), "+v"(pf[2]), "+v"(pf[3]), "+v"(gv[0]), "+v"(gv[1]),
                   "+v"(gv[2]), "+v"(gv[3])::"memory");
      BAR();
      asm volatile("" ::: "memory");
      bf16x8 kf[3];
#pragma unroll
      for (int et = 0; et < 3; ++et) kf[et] = lds_b128(ksm + (et * 64 + lane) * 16);
#pragma unroll
      for (int m = 0; m < 4; ++m) {
        const f32x4 z = {0.f, 0.f, 0.f, 0.f};
        f32x4 n0 = MFMA(kf[0], pf[m], z), n1 = MFMA(kf[1], pf[m], z), dn = MFMA(kf[2], pf[m], z);
        const float sc = gv[m] * __builtin_amdgcn_rcpf(dn[0] + 1e-6f);
        n0 *= sc;
        n1 *= sc;
        u32x4 v = {pack2(n0[0], n0[1]), pack2(n0[2], n0[3]), pack2(n1[0], n1[1]), pack2(n1[2], n1[3])};
        char* dst = p.a2 + ((size_t)(512 * ro + 64 * lw + 16 * m + l15) * KA2 + FF + 32 * hd + 8 * g) * 2;
        asm volatile("global_store_dwordx4 %0, %1, off sc1\n\ts_nop 1" ::"v"(dst), "v"(v) : "memory");
      }
    } else {
      WAIT_V(12);
      BAR();
      WAIT_V(8);
    }
    BAR();
    for (int j = 0; j < ns - 1; ++j) {
      if (j + 4 < ns) { stage(j + 4); WAIT_V(8); }
      else if (j + 4 == ns) WAIT_V(4);
      else WAIT_V(0);
      BAR();
    }
    return;
  }
  {
    f32x4 omg;
#pragma unroll
    for (int m = 0; m < 4; ++m) omg[m] = p.gbuf[SEQ + 128 * tm + 64 * wm + 16 * m + l15];
    *(__attribute__((address_space(3))) f32x4*)(uintptr_t)(ksm + 3072 + threadIdx.x * 16) = omg;
  }
  f32x4 acc[4][4];
#pragma unroll
  for (int m = 0; m < 4; ++m)
#pragma unroll
    for (int n = 0; n < 4; ++n) acc[m][n] = (f32x4){0.f, 0.f, 0.f, 0.f};
  unsigned rA[2], rB[4];
  rA[0] = sbase + (64 * wm + l15) * 128 + ((g ^ (l15 >> 1)) * 16);
  rA[1] = rA[0] ^ 64;
  {
    const int i = (g & 1) * 4 + (l15 >> 2), pp = lane & 3;
#pragma unroll
    for (int tp = 0; tp < 4; ++tp)
      rB[tp] = sbase + 65536 + ((g >> 1) * 2 + wn) * 1024 + i * 128 + ((tp ^ (i >> 1)) * 2 + (pp >> 1)) * 16 + (pp & 1) * 8;
  }
  PIN_ACC();
  asm volatile("s_waitcnt vmcnt(0) lgkmcnt(0)" : "+v"(rA[0]), "+v"(rA[1]), "+v"(rB[0]), "+v"(rB[1]), "+v"(rB[2]), "+v"(rB[3])::"memory");
  const int ng = s ? K2_NH / 4 : K2_G0;
  const unsigned endm = (1u << (ng - 1)) | (w == 0 ? (s ? 1u : (7u << (K2_G0 - 4))) : 0u);
  BAR();
  bf16x8 a0[4] = {}, a1[4] = {};
  s16x4 l0[4] = {}, h0[4] = {}, l1[4] = {}, h1[4] = {};
#pragma unroll
  for (int q = 0; q < 4; ++q) {
    LDS_RD(a0[q], rA[0], q * 2048);
    LDS_TR(l0[q], rB[q], 0);
    LDS_TR(h0[q], rB[q], 4096);
  }
  WAIT_FRAG(a0, l0, h0);
#define K2_READ(r, a, l, h, u, kh)                                                                          \
  do {                                                                                                      \
    if ((r) % 3 == 0) LDS_RD(a[(r) / 3], rA[kh], (u) * 16384 + ((r) / 3) * 2048); \
    else if ((r) % 3 == 1) LDS_TR2(l[(r) / 3], rB[(r) / 3], (u) * 16384 + (kh) * 8192); \
    else LDS_TR2(h[(r) / 3], rB[(r) / 3], (u) * 16384 + (kh) * 8192 + 4096); \
  } while (0)
  auto group = [&]() __attribute__((always_inline)) {
#pragma unroll
    for (int u = 0; u < 4; ++u) {
      SCHED();
#pragma unroll
      for (int t = 0; t < 16; ++t) {
        MFMA_ACC(acc[t & 3][t >> 2], cat8(l0[t >> 2], h0[t >> 2]), a0[t & 3]);
        SCHED();
#pragma unroll
        for (int r = t * K2_RS; r < (t + 1) * K2_RS; ++r)
          if (r < 12) K2_READ(r, a1, l1, h1, u, 1);
        SCHED();
      }
      WAIT_FRAG(a1, l1, h1);
      BAR();
      SCHED();
#pragma unroll
      for (int t = 0; t < 16; ++t) {
        MFMA_ACC(acc[t & 3][t >> 2], cat8(l1[t >> 2], h1[t >> 2]), a1[t & 3]);
        SCHED();
#pragma unroll
        for (int r = t * K2_RS; r < (t + 1) * K2_RS; ++r)
          if (r < 12) K2_READ(r, a0, l0, h0, (u + 1) & 3, 0);
        SCHED();
      }
      WAIT_FRAG(a0, l0, h0);
    }
    SCHED();
  };
  for (int jg = 0;;) {
    group();
    if (!((endm >> jg++) & 1u)) continue;
    if (jg == ng) break;
    if (s) {
      if (threadIdx.x == 0) __hip_atomic_fetch_add((gu32*)(p.flags + ro * 32), 1u, __ATOMIC_RELAXED, __HIP_MEMORY_SCOPE_AGENT);
    } else {
      gu32* f = (gu32*)(p.flags + (tm >> 2) * 32);
      if (jg == K2_G0 - 3) {
        asm volatile("s_mov_b32 m0, %1\n\ts_nop 0\n\tglobal_load_lds_dword %0, off sc1" ::"v"(f), "s"(ksm) : "memory");
      } else if (jg == K2_G0 - 2) {
        WAIT_V(0);
        if (*(volatile __attribute__((address_space(3))) unsigned*)(uintptr_t)ksm < 32u)
          while (__hip_atomic_load(f, __ATOMIC_RELAXED, __HIP_MEMORY_SCOPE_AGENT) < 32u) __builtin_amdgcn_s_sleep(2);
        asm volatile("buffer_inv sc1" ::: "memory");
      } else {
        WAIT_V(0);
      }
    }
  }
  int tid = threadIdx.x;
  asm volatile("s_nop 7\n\ts_nop 7" : "+v"(tid));
  PIN_ACC();
  {
    const f32x4 omg = *(__attribute__((address_space(3))) f32x4*)(uintptr_t)(ksm + 3072 + tid * 16);
#pragma unroll
    for (int m = 0; m < 4; ++m)
#pragma unroll
      for (int n = 0; n < 4; ++n) acc[m][n] *= omg[m];
  }
  if (!s) {
    PIN_ACC();
    asm volatile("s_nop 3");
#pragma nounroll
    for (int jg = 0; jg < 4; ++jg) group();
    asm volatile("s_nop 7\n\ts_nop 7" : "+v"(tid));
    PIN_ACC();
  }
  {
    const int lane = tid & 63, g = lane >> 4, l15 = lane & 15;
    gu32* f = (gu32*)(p.flags + 256 + tile);
    if (s) {
      float* xw = p.xch + ((size_t)tile * 4 + w) * 4096 + lane * 4;
#pragma unroll
      for (int q = 0; q < 16; ++q)
        asm volatile("global_store_dwordx4 %0, %1, off sc1\n\ts_nop 1" ::"v"(xw + q * 256), "v"(acc[q >> 2][q & 3]) : "memory");
      WAIT_V(0);
      BAR();
      if (tid == 0) __hip_atomic_store(f, 1u, __ATOMIC_RELAXED, __HIP_MEMORY_SCOPE_AGENT);
    } else {
      while (__hip_atomic_load(f, __ATOMIC_RELAXED, __HIP_MEMORY_SCOPE_AGENT) == 0u) __builtin_amdgcn_s_sleep(1);
      const __amdgpu_buffer_rsrc_t xr =
          __builtin_amdgcn_make_buffer_rsrc((void*)(p.xch + ((size_t)tile * 4 + w) * 4096), 0, 16384, 0x00020000);
      f32x4 o[16];
#pragma unroll
      for (int q = 0; q < 16; ++q)
        o[q] = __builtin_bit_cast(f32x4, __builtin_amdgcn_raw_buffer_load_b128(xr, lane * 16 + q * 1024, 0, 16));
#pragma unroll
      for (int q = 0; q < 8; ++q) {
        const int m = q >> 1, n = (q & 1) * 2;
        const f32x4 x = acc[m][n] + o[2 * q], y = acc[m][n + 1] + o[2 * q + 1];
        const auto r0 = __builtin_amdgcn_permlane16_swap(pack2(x[0], x[1]), pack2(y[0], y[1]), false, false);
        const auto r1 = __builtin_amdgcn_permlane16_swap(pack2(x[2], x[3]), pack2(y[2], y[3]), false, false);
        const u32x4 pk = {r0[0], r1[0], r0[1], r1[1]};
        const int row = 128 * tm + 64 * wm + 16 * m + l15;
        *(u32x4*)(p.out + ((size_t)row * DM + 128 * tn + 64 * wn + 16 * n + 16 * (g & 1) + 8 * (g >> 1)) * 2) = pk;
      }
    }
  }
}

extern "C" void kernel_launch(void* const* d_in, const int* in_sizes, int n_in, void* d_out, int out_size, void* d_ws,
                              size_t ws_size, hipStream_t stream) {
  char* ws = (char*)d_ws;
  unsigned* flags = (unsigned*)ws;
  float* gbuf = (float*)(ws + 4096);
  float* kvp = (float*)(ws + 65536);
  char* pq = ws + (2u << 20);
  char* a2 = ws + (8u << 20);
  float* xch = (float*)(ws + (32u << 20));
  K1Args a;
  a.x = (const char*)d_in[0]; a.wqkv = (const char*)d_in[1]; a.wg = (const char*)d_in[4]; a.wu = (const char*)d_in[5];
  a.gate_w = (const char*)d_in[7]; a.a2 = a2; a.pq = pq; a.kvp = kvp; a.gbuf = gbuf; a.flags = flags;
  a.wd = (const char*)d_in[6]; a.wo = (const char*)d_in[2];
  k1_kernel<<<256, 512, 0, stream>>>(a);
  K2Args b;
  b.a2 = a2; b.wd = (const char*)d_in[6]; b.wo = (const char*)d_in[2]; b.gbuf = gbuf; b.pq = pq; b.kvp = kvp; b.xch = xch; b.flags = flags;
  b.out = (char*)d_out;
  k2_kernel<<<256, 768, 0, stream>>>(b);
}
```
